```python
import torch

def _softmax(x, axis=-1):
    return torch.softmax(x, dim=axis)
def _gelu(x):
    return torch.nn.functional.gelu(x, approximate="tanh")
def _silu(x):
    return torch.nn.functional.silu(x)
def _sigmoid(x):
    return torch.sigmoid(x)
def _layernorm(x, axis=-1, eps=1e-5):
    m = x.mean(dim=axis, keepdim=True)
    v = x.var(dim=axis, keepdim=True, unbiased=False)
    return (x - m) / torch.sqrt(v + eps)
def _scatter_add(tgt, idx, src, axis=0):
    return tgt.clone().index_add_(axis, idx, src)
def _segment_sum(src, seg, n):
    out = torch.zeros((n,) + src.shape[1:], dtype=src.dtype)
    return out.index_add_(0, seg, src)
def _pad(x, pw):
    flat = tuple(v for pair in reversed(pw) for v in pair)
    return torch.nn.functional.pad(x, flat)

kernel_name = 'gated_mix__linear_kernel_hedgehog__softmax_full_mha__prompt'

def setup_inputs():

    _w = (lambda a: (lambda t: t.to(torch.bfloat16) if t.is_floating_point() else t)(torch.as_tensor(a)))
    def _A_setup():
        B, S, D, H = 1, 2048, 512, 32
        Dh = D // H
        return {
            "x": _w((lambda *s: torch.randn(*s, dtype=torch.bfloat16) * 0.05)(B, S, D)),
            "wqkv": _w((lambda *s: torch.randn(*s, dtype=torch.bfloat16) * 3.2)(D, 3 * D)),
            "w_phi": _w((lambda *s: torch.randn(*s, dtype=torch.bfloat16) * 3.2)(H, Dh, Dh) * (Dh ** -0.5)),
            "wo": _w((lambda *s: torch.randn(*s, dtype=torch.bfloat16) * 0.05)(D, D)),
        }
    _A_in = _A_setup()

    def _B_setup():
        B, S, D = 1, 2048, 512
        return {
            "x": _w((lambda *s: torch.randn(*s, dtype=torch.bfloat16) * 0.05)(B, S, D)),
            "wqkv": _w((lambda *s: torch.randn(*s, dtype=torch.bfloat16) * 1.6)(D, 3 * D)),
            "wo": _w((lambda *s: torch.randn(*s, dtype=torch.bfloat16) * 0.05)(D, D)),
        }
    _B_in = _B_setup()

    return {
        "A_x": _A_in["x"],
        "A_wqkv": _A_in["wqkv"],
        "A_w_phi": _A_in["w_phi"],
        "A_wo": _A_in["wo"],
        "B_x": _B_in["x"],
        "B_wqkv": _B_in["wqkv"],
        "B_wo": _B_in["wo"],
        "gate_w": _w((lambda *s: torch.randn(*s, dtype=torch.bfloat16) * 0.05)(512) * 16.0),
    }

def reference(A_x, A_wqkv, A_w_phi, A_wo, B_x, B_wqkv, B_wo, gate_w):
    def _A_ref(x, wqkv, w_phi, wo):
        B, S, D = x.shape
        H = 32; Dh = D // H
        qkv = (lambda x, *d: x.permute(*d))((x @ wqkv).reshape(B, S, 3, H, Dh), 2, 0, 3, 1, 4)
        q, k, v = qkv[0], qkv[1], qkv[2]
        pq = torch.einsum("bhsd,hdf->bhsf", q, w_phi)
        pk = torch.einsum("bhsd,hdf->bhsf", k, w_phi)
        phi_q = torch.concatenate([_softmax(pq, axis=-1), _softmax(-pq, axis=-1)], axis=-1)
        phi_k = torch.concatenate([_softmax(pk, axis=-1), _softmax(-pk, axis=-1)], axis=-1)
        kv = torch.einsum("bhsf,bhsd->bhfd", phi_k, v)
        z  = torch.einsum("bhsf->bhf", phi_k)
        num = torch.einsum("bhqf,bhfd->bhqd", phi_q, kv)
        den = torch.einsum("bhqf,bhf->bhq", phi_q, z)[..., None] + 1e-6
        o = num / den
        return (lambda x, *d: x.permute(*d))(o, 0, 2, 1, 3).reshape(B, S, D) @ wo

    def _B_ref(x, wqkv, wo):
        B, S, D = x.shape
        H = 32; Dh = D // H
        qkv = (lambda x, *d: x.permute(*d))((x @ wqkv).reshape(B, S, 3, H, Dh), 2, 0, 3, 1, 4)
        q, k, v = qkv[0], qkv[1], qkv[2]
        scale = Dh ** -0.5
        scores = torch.einsum("bhqd,bhkd->bhqk", q, k) * scale
        p = _softmax(scores, axis=-1)
        o = torch.einsum("bhqk,bhkd->bhqd", p, v)
        return (lambda x, *d: x.permute(*d))(o, 0, 2, 1, 3).reshape(B, S, D) @ wo

    x = A_x
    g = _sigmoid((x * gate_w).sum(-1, keepdims=True))
    a = _A_ref(x, A_wqkv, A_w_phi, A_wo)
    b = _B_ref(x, B_wqkv, B_wo)
    return g * a + (1 - g) * b
```

```cpp
#include <hip/hip_runtime.h>
#include <hip/hip_bf16.h>
#include <stdint.h>

typedef __attribute__((ext_vector_type(2))) __bf16 bf16x2;
typedef __attribute__((ext_vector_type(8))) __bf16 bf16x8;
typedef __attribute__((ext_vector_type(16))) __bf16 bf16x16;
typedef __attribute__((ext_vector_type(2))) float f32x2;
typedef __attribute__((ext_vector_type(4))) float f32x4;
typedef __attribute__((ext_vector_type(8))) float f32x8;
typedef __attribute__((ext_vector_type(16))) float f32x16;
typedef __attribute__((ext_vector_type(2))) unsigned u32x2;
typedef __attribute__((ext_vector_type(4))) unsigned u32x4;
typedef __attribute__((ext_vector_type(4))) short s16x4;
#define LAS __attribute__((address_space(3)))
#define GAS __attribute__((address_space(1)))
#define DI __device__ __forceinline__
#define MFMA(a, b, c) __builtin_amdgcn_mfma_f32_32x32x16_bf16((a), (b), (c), 0, 0, 0)

constexpr int SEQ = 2048, DM = 512, NH = 32;
constexpr float LOG2E = 1.4426950408889634f;
constexpr size_t WS_QB = 0, WS_KB = 2u << 20, WS_VB = 4u << 20, WS_PHQ = 6u << 20, WS_KVP = 10u << 20, WS_OB = 12u << 20,
                 WS_WOF = 16u << 20, WS_G = 17u << 20;

DI unsigned pack2(float a, float b) { f32x2 v = {a, b}; return __builtin_bit_cast(unsigned, __builtin_convertvector(v, bf16x2)); }
DI float rbf(float a) { return (float)(__bf16)a; }
DI float bf2f(unsigned short b) { return __uint_as_float((unsigned)b << 16); }
DI u32x4 pack8(const f32x16& x, int o, float sc) {
    u32x4 p; p[0] = pack2(x[o] * sc, x[o + 1] * sc); p[1] = pack2(x[o + 2] * sc, x[o + 3] * sc); p[2] = pack2(x[o + 4] * sc, x[o + 5] * sc); p[3] = pack2(x[o + 6] * sc, x[o + 7] * sc); return p; }
DI u32x4 pack8(const f32x16& x, int o) {
    u32x4 p; p[0] = pack2(x[o], x[o + 1]); p[1] = pack2(x[o + 2], x[o + 3]); p[2] = pack2(x[o + 4], x[o + 5]); p[3] = pack2(x[o + 6], x[o + 7]); return p; }
DI bf16x8 asbf(u32x4 v) { return __builtin_bit_cast(bf16x8, v); }
DI float swap_max(float m) { auto r = __builtin_amdgcn_permlane32_swap(__float_as_uint(m), __float_as_uint(m), false, false); return fmaxf(__uint_as_float(r[0]), __uint_as_float(r[1])); }
DI float swap_add(float m) { auto r = __builtin_amdgcn_permlane32_swap(__float_as_uint(m), __float_as_uint(m), false, false); return __uint_as_float(r[0]) + __uint_as_float(r[1]); }
DI void glds16(const void* g, LAS char* l) { __builtin_amdgcn_global_load_lds((const GAS unsigned*)g, (LAS unsigned*)l, 16, 0, 0); }
typedef short v4i16_t __attribute__((ext_vector_type(4)));
DI s16x4 trread(const LAS char* p) { return __builtin_bit_cast(s16x4, __builtin_amdgcn_ds_read_tr16_b64_v4i16((LAS v4i16_t*)p)); }
DI bf16x8 tr2(const LAS char* p0, const LAS char* p1) { s16x4 a = trread(p0), b = trread(p1); return __builtin_bit_cast(bf16x8, __builtin_shufflevector(a, b, 0, 1, 2, 3, 4, 5, 6, 7)); }
#define WAIT_BAR(N) asm volatile("s_waitcnt vmcnt(" #N ") lgkmcnt(0)\n\ts_barrier" ::: "memory")

DI void hedgehog(const f32x16& pT, f32x16& ph) {
    float x[8];
#pragma unroll
    for (int i = 0; i < 8; ++i) x[i] = rbf(pT[i]) * LOG2E;
    float mx = fmaxf(fmaxf(fmaxf(x[0], x[1]), fmaxf(x[2], x[3])), fmaxf(fmaxf(x[4], x[5]), fmaxf(x[6], x[7])));
    float mn = fminf(fminf(fminf(x[0], x[1]), fminf(x[2], x[3])), fminf(fminf(x[4], x[5]), fminf(x[6], x[7])));
    mx = swap_max(mx); mn = -swap_max(-mn);
    float sp = 0.f, sn = 0.f;
#pragma unroll
    for (int i = 0; i < 8; ++i) { ph[i] = __builtin_amdgcn_exp2f(x[i] - mx); ph[8 + i] = __builtin_amdgcn_exp2f(mn - x[i]); sp += ph[i]; sn += ph[8 + i]; }
    sp = __builtin_amdgcn_rcpf(swap_add(sp)); sn = __builtin_amdgcn_rcpf(swap_add(sn));
#pragma unroll
    for (int i = 0; i < 8; ++i) { ph[i] *= sp; ph[8 + i] *= sn; }
}

constexpr int K1_STAGE = 40960, K1_LDS = 4 * K1_STAGE;
__global__ __launch_bounds__(512, 2) void k1_qkv(const unsigned short* __restrict__ x, const unsigned short* __restrict__ wA, const unsigned short* __restrict__ wB,
                                                const unsigned short* __restrict__ wphi, const unsigned short* __restrict__ woA, const unsigned short* __restrict__ woB,
                                                const unsigned short* __restrict__ gatew, char* __restrict__ ws) {
    extern __shared__ __attribute__((aligned(16))) char smem[];
    LAS char* lds = (LAS char*)smem;
    const int tid = threadIdx.x, lane = tid & 63, w = __builtin_amdgcn_readfirstlane(tid >> 6), r = lane & 31, h = lane >> 5;
    const int xcd = blockIdx.x & 7, bi = blockIdx.x >> 3;
    const int rb = (xcd & 1) * 8 + (bi & 7), cg = (xcd >> 1) * 4 + (bi >> 3);
    const bool isB = cg >= 8; const int hg = cg & 7;
    const unsigned short* W = isB ? wB : wA;
    const int wt = w & 3, wh = w >> 2;
    unsigned short side[16];
    const int bidx = rb * 8 + hg;
    u32x4 gx0, gx1, gw0;
    if (isB) {
        const int fid = bidx * 8 + w, ks = fid >> 4, nt = fid & 15;
        const unsigned short* wo = (ks < 32 ? woA : woB) + (size_t)((ks & 31) * 16 + 4 * h) * DM + nt * 32 + r;
#pragma unroll
        for (int j = 0; j < 8; ++j) side[j] = wo[(size_t)((j >> 2) * 8 + (j & 3)) * DM];
        gx0 = *(const u32x4*)(x + (size_t)(bidx * 16 + 2 * w) * DM + lane * 8);
        gx1 = *(const u32x4*)(x + (size_t)(bidx * 16 + 2 * w + 1) * DM + lane * 8);
        gw0 = *(const u32x4*)(gatew + lane * 8);
    } else {
#pragma unroll
        for (int hh = 0; hh < 2; ++hh)
#pragma unroll
            for (int j = 0; j < 8; ++j) side[hh * 8 + j] = wphi[((hg * 4 + wh * 2 + hh) * 16 + (j >> 2) * 8 + 4 * h + (j & 3)) * 16 + (r & 15)];
    }
    const unsigned short* xs[2]; const unsigned short* wsrc[3];
#pragma unroll
    for (int i = 0; i < 2; ++i) { const int row = (2 * w + i) * 8 + (lane >> 3); xs[i] = x + (size_t)(rb * 128 + row) * DM + (((lane & 7) ^ ((row >> 1) & 7)) * 8); }
#pragma unroll
    for (int i = 0; i < 3; ++i) { const int p = 3 * w + i, seg = p >> 3, kr = 8 * (p & 7) + (lane >> 3);
        wsrc[i] = W + (size_t)kr * 1536 + seg * 512 + hg * 64 + (((lane & 7) ^ (((kr >> 1) & 1) << 2)) * 8); }
#define K1_STAGE_LOAD(kt) do { LAS char* sb_ = lds + ((kt) & 3) * K1_STAGE; \
        glds16(xs[0] + (kt) * 64, sb_ + (2 * w) * 1024); glds16(xs[1] + (kt) * 64, sb_ + (2 * w + 1) * 1024); \
        glds16(wsrc[0] + (size_t)(kt) * 64 * 1536, sb_ + 16384 + (3 * w) * 1024); glds16(wsrc[1] + (size_t)(kt) * 64 * 1536, sb_ + 16384 + (3 * w + 1) * 1024); \
        glds16(wsrc[2] + (size_t)(kt) * 64 * 1536, sb_ + 16384 + (3 * w + 2) * 1024); } while (0)
    K1_STAGE_LOAD(0); K1_STAGE_LOAD(1); K1_STAGE_LOAD(2); K1_STAGE_LOAD(3);
    int xoff[4];
#pragma unroll
    for (int s = 0; s < 4; ++s) xoff[s] = (32 * wt + r) * 128 + (((2 * s + h) ^ ((r >> 1) & 7)) << 4);
    const int blk = (lane >> 4) & 1, q4 = (lane & 15) >> 2, p4 = lane & 3;
    const int woff = 16384 + (8 * h + q4) * 128 + (((4 * wh + 2 * blk + (p4 >> 1)) ^ ((q4 >> 1) << 2)) << 4) + 8 * (p4 & 1);
    f32x16 aq = {}, ak = {}, av = {};
#define K1_COMPUTE(kt) do { const LAS char* sb_ = lds + ((kt) & 3) * K1_STAGE; \
        _Pragma("unroll") for (int s = 0; s < 4; ++s) { \
            const bf16x8 xf = *(const LAS bf16x8*)(sb_ + xoff[s]); \
            const LAS char* wp_ = sb_ + woff + s * 2048; \
            const bf16x8 fq = tr2(wp_, wp_ + 512), fk = tr2(wp_ + 8192, wp_ + 8192 + 512), fv = tr2(wp_ + 16384, wp_ + 16384 + 512); \
            aq = MFMA(fq, xf, aq); ak = MFMA(fk, xf, ak); av = MFMA(xf, fv, av); } } while (0)
    WAIT_BAR(15); K1_COMPUTE(0);
    WAIT_BAR(10); K1_STAGE_LOAD(4); K1_COMPUTE(1);
    WAIT_BAR(10); K1_STAGE_LOAD(5); K1_COMPUTE(2);
    WAIT_BAR(10); K1_STAGE_LOAD(6); K1_COMPUTE(3);
    WAIT_BAR(10); K1_STAGE_LOAD(7); K1_COMPUTE(4);
    WAIT_BAR(10); K1_COMPUTE(5);
    WAIT_BAR(5); K1_COMPUTE(6);
    WAIT_BAR(0); K1_COMPUTE(7);
    const int tile = rb * 4 + wt;
    const int head0 = hg * 4 + wh * 2;
    if (isB) {
        const int sl = (r & 0x13) | ((r & 4) << 1) | ((r & 8) >> 1);
#pragma unroll
        for (int hh = 0; hh < 2; ++hh) {
            const size_t fo = ((size_t)(head0 + hh) * 64 + tile) * 1024;
            *(u32x4*)(ws + WS_QB + fo + lane * 16) = pack8(aq, 8 * hh, 0.25f * LOG2E);
            *(u32x4*)(ws + WS_KB + fo + (sl + 32 * h) * 16) = pack8(ak, 8 * hh);
        }
        { const size_t fo = ((size_t)(head0 + (r >> 4)) * 64 + tile) * 1024 + h * 256 + (r & 15) * 16;
          *(u32x4*)(ws + WS_VB + fo) = pack8(av, 0); *(u32x4*)(ws + WS_VB + fo + 512) = pack8(av, 8); }
        { u32x4 f; f[0] = side[0] | ((unsigned)side[1] << 16); f[1] = side[2] | ((unsigned)side[3] << 16); f[2] = side[4] | ((unsigned)side[5] << 16); f[3] = side[6] | ((unsigned)side[7] << 16);
          *(u32x4*)(ws + WS_WOF + (size_t)(bidx * 8 + w) * 1024 + lane * 16) = f; }
        float s0 = 0.f, s1 = 0.f;
#pragma unroll
        for (int j = 0; j < 4; ++j) {
            const float wl = __uint_as_float(gw0[j] << 16), wu = __uint_as_float(gw0[j] & 0xffff0000u);
            s0 += rbf(__uint_as_float(gx0[j] << 16) * wl) + rbf(__uint_as_float(gx0[j] & 0xffff0000u) * wu);
            s1 += rbf(__uint_as_float(gx1[j] << 16) * wl) + rbf(__uint_as_float(gx1[j] & 0xffff0000u) * wu);
        }
#pragma unroll
        for (int o = 1; o < 64; o <<= 1) { s0 += __shfl_xor(s0, o); s1 += __shfl_xor(s1, o); }
        if (lane < 2) { const float t = rbf(lane ? s1 : s0); const float g = rbf(1.f / (1.f + __expf(-t)));
            f32x2 gg = {g, rbf(1.f - g)}; *(f32x2*)(ws + WS_G + (size_t)(bidx * 16 + 2 * w + lane) * 8) = gg; }
    } else {
        LAS char* img = lds + w * 2048;
        LAS char* red = lds + 16384;
        const u32x4 vp0 = pack8(av, 0), vp1 = pack8(av, 8);
        const f32x16 zero = {};
#pragma unroll
        for (int hh = 0; hh < 2; ++hh) {
            u32x4 wf; wf[0] = side[8 * hh + 0] | ((unsigned)side[8 * hh + 1] << 16); wf[1] = side[8 * hh + 2] | ((unsigned)side[8 * hh + 3] << 16);
            wf[2] = side[8 * hh + 4] | ((unsigned)side[8 * hh + 5] << 16); wf[3] = side[8 * hh + 6] | ((unsigned)side[8 * hh + 7] << 16);
            f32x16 ph;
            f32x16 pq = MFMA(asbf(wf), asbf(pack8(aq, 8 * hh)), zero);
            f32x16 pk = MFMA(asbf(wf), asbf(pack8(ak, 8 * hh)), zero);
            hedgehog(pq, ph);
            const size_t fo = ((size_t)(head0 + hh) * 64 + tile) * 2048 + lane * 16;
            *(u32x4*)(ws + WS_PHQ + fo) = pack8(ph, 0); *(u32x4*)(ws + WS_PHQ + fo + 1024) = pack8(ph, 8);
            hedgehog(pk, ph);
#pragma unroll
            for (int g = 0; g < 4; ++g) { u32x2 v = {pack2(ph[4 * g], ph[4 * g + 1]), pack2(ph[4 * g + 2], ph[4 * g + 3])}; *(LAS u32x2*)(img + r * 64 + 8 * (2 * g + h)) = v; }
            __builtin_amdgcn_fence(__ATOMIC_RELEASE, "wavefront"); __builtin_amdgcn_wave_barrier(); __builtin_amdgcn_fence(__ATOMIC_ACQUIRE, "wavefront");
            const LAS char* tp = img + (4 * h + q4) * 64 + blk * 32 + p4 * 8;
            const bf16x8 a0 = tr2(tp, tp + 8 * 64), a1 = tr2(tp + 16 * 64, tp + 24 * 64);
            const bool mine = (r >> 4) == hh;
            u32x4 b0, b1;
#pragma unroll
            for (int j = 0; j < 4; ++j) { b0[j] = mine ? vp0[j] : 0x3F803F80u; b1[j] = mine ? vp1[j] : 0x3F803F80u; }
            f32x16 kv = MFMA(a0, asbf(b0), zero); kv = MFMA(a1, asbf(b1), kv);
            LAS char* rp = red + (((wh * 2 + hh) * 4 + wt) * 4096) + (lane ^ (hh << 4)) * 16;
#pragma unroll
            for (int g = 0; g < 4; ++g) { f32x4 v = {kv[4 * g], kv[4 * g + 1], kv[4 * g + 2], kv[4 * g + 3]}; *(LAS f32x4*)(rp + g * 1024) = v; }
            __builtin_amdgcn_fence(__ATOMIC_RELEASE, "wavefront"); __builtin_amdgcn_wave_barrier(); __builtin_amdgcn_fence(__ATOMIC_ACQUIRE, "wavefront");
        }
        __syncthreads();
#pragma unroll
        for (int i = 0; i < 2; ++i) {
            const int e = tid + i * 512, hd = e >> 8, o = (e & 255) * 16;
            const LAS char* rp = red + hd * 16384 + o;
            const f32x4 v = (*(const LAS f32x4*)(rp) + *(const LAS f32x4*)(rp + 4096)) + (*(const LAS f32x4*)(rp + 8192) + *(const LAS f32x4*)(rp + 12288));
            *(f32x4*)(ws + WS_KVP + ((size_t)(hg * 4 + hd) * 16 + rb) * 4096 + o) = v;
        }
    }
}

constexpr int K2_VSTR = 1120, K2_V = 65536, K2_KVA = K2_V + 64 * K2_VSTR, K2_Q = K2_KVA + 2048, K2_LDS = K2_Q + 8192;
DI f32x16 smf(bf16x8 a, const f32x16& p, int hi, f32x16 c) {
    f32x8 b = hi ? __builtin_shufflevector(p, p, 8, 9, 10, 11, 12, 13, 14, 15) : __builtin_shufflevector(p, p, 0, 1, 2, 3, 4, 5, 6, 7);
    return __builtin_amdgcn_smfmac_f32_32x32x32_bf16(a, __builtin_bit_cast(bf16x16, b), c, (int)0xDDDDDDDD, 0, 0);
}
DI float max16(const f32x16& s) {
    float a = fmaxf(fmaxf(s[0], s[1]), s[2]), b = fmaxf(fmaxf(s[3], s[4]), s[5]);
    a = fmaxf(fmaxf(a, s[6]), s[7]); b = fmaxf(fmaxf(b, s[8]), s[9]); a = fmaxf(fmaxf(a, s[10]), s[11]); b = fmaxf(fmaxf(b, s[12]), s[13]);
    a = fmaxf(fmaxf(a, s[14]), s[15]); return fmaxf(a, b);
}
__global__ __launch_bounds__(512, 2) void k2_attn(char* __restrict__ ws) {
    extern __shared__ __attribute__((aligned(16))) char smem[];
    LAS char* lds = (LAS char*)smem;
    const int tid = threadIdx.x, lane = tid & 63, w = __builtin_amdgcn_readfirstlane(tid >> 6), r = lane & 31, h = lane >> 5;
    const int xcd = blockIdx.x & 7, bi = blockIdx.x >> 3, head = xcd * 4 + (bi >> 3), qt = (bi & 7) * 8 + w;
    const char* kg = ws + WS_KB + (size_t)head * 65536 + w * 1024 + lane * 16;
    const char* vg = ws + WS_VB + (size_t)head * 65536 + w * 1024 + lane * 16;
    glds16(ws + WS_QB + ((size_t)head * 64 + qt) * 1024 + lane * 16, lds + K2_Q + w * 1024);
#pragma unroll
    for (int c = 0; c < 8; ++c) { glds16(kg + c * 8192, lds + (c * 8 + w) * 1024); glds16(vg + c * 8192, lds + K2_V + (c * 8 + w) * K2_VSTR); }
    if (tid < 256) { const int t = tid >> 2, k = tid & 3; const unsigned v = (k & 1) ? 0u : 0x3F803F80u; u32x4 vv = {v, v, v, v};
        *(LAS u32x4*)(lds + K2_V + t * K2_VSTR + 1024 + (k & 1) * 16 + (k >> 1) * 64) = vv; }
    const bool g2 = (r >= 20 && r < 28);
    const int padoff = 1024 + (g2 ? 64 : 0) + ((r == 16 || r == 20) ? 0 : 16);
    const LAS char* kp = lds + lane * 16;
    const LAS char* vp0 = lds + K2_V + (r < 16 ? h * 256 + r * 16 : padoff);
    const LAS char* vp1 = lds + K2_V + (r < 16 ? 512 + h * 256 + r * 16 : padoff);
    WAIT_BAR(14);
    const bf16x8 qf = *(const LAS bf16x8*)(lds + K2_Q + w * 1024 + lane * 16);
    f32x16 negm, o = {}, sc, sn;
    { const f32x16 zero = {}; const f32x16 s0 = MFMA(*(const LAS bf16x8*)kp, qf, zero); const float m = swap_max(max16(s0));
#pragma unroll
      for (int i = 0; i < 16; ++i) negm[i] = -m; }
#define KF(t) (*(const LAS bf16x8*)(kp + (t) * 1024))
#define VF0(t) (*(const LAS bf16x8*)(vp0 + (t) * K2_VSTR))
#define VF1(t) (*(const LAS bf16x8*)(vp1 + (t) * K2_VSTR))
#define STEP(C, N, t, tn) do { const bf16x8 v0_ = VF0(t), v1_ = VF1(t); N = MFMA(KF(tn), qf, negm); \
        _Pragma("unroll") for (int i = 0; i < 8; ++i) C[i] = __builtin_amdgcn_exp2f(C[i]); \
        o = smf(v0_, C, 0, o); \
        _Pragma("unroll") for (int i = 8; i < 16; ++i) C[i] = __builtin_amdgcn_exp2f(C[i]); \
        o = smf(v1_, C, 1, o); } while (0)
#define RUN(t0, t1) do { _Pragma("unroll 2") for (int t = (t0); t < (t1); t += 2) { STEP(sc, sn, t, t + 1); STEP(sn, sc, t + 1, (t + 2 < 64 ? t + 2 : 63)); } } while (0)
    sc = MFMA(KF(0), qf, negm);
    RUN(0, 6);
    WAIT_BAR(10);
    RUN(6, 22);
    WAIT_BAR(0);
    f32x2 part[16];
#pragma unroll
    for (int i = 0; i < 16; ++i) part[i] = *(const f32x2*)(ws + WS_KVP + ((size_t)head * 16 + i) * 4096 + tid * 8);
    const u32x4 ph0 = *(const u32x4*)(ws + WS_PHQ + ((size_t)head * 64 + qt) * 2048 + lane * 16);
    const u32x4 ph1 = *(const u32x4*)(ws + WS_PHQ + ((size_t)head * 64 + qt) * 2048 + 1024 + lane * 16);
    RUN(22, 38);
    { f32x2 a = ((part[0] + part[1]) + (part[2] + part[3])) + ((part[4] + part[5]) + (part[6] + part[7]));
      f32x2 b = ((part[8] + part[9]) + (part[10] + part[11])) + ((part[12] + part[13]) + (part[14] + part[15]));
      a += b;
      const int g = tid >> 7, ln = (tid & 127) >> 1, pr = tid & 1;
      *(LAS unsigned*)(lds + K2_KVA + (g >> 1) * 1024 + ln * 16 + ((g & 1) * 2 + pr) * 4) = pack2(a[0], a[1]); }
    RUN(38, 64);
    float l = o[8];
    {
        float chk = fabsf(o[0]);
#pragma unroll
        for (int i = 1; i < 8; ++i) chk = fmaxf(chk, fabsf(o[i]));
        const bool bad = !(l < 1.8446744e19f) || !(chk < 3.0e38f);
        if (__builtin_expect(__any(bad), 0)) {
            const f32x16 zero = {}; float m = -INFINITY;
            for (int t = 0; t < 64; ++t) { const f32x16 s0 = MFMA(KF(t), qf, zero); m = fmaxf(m, max16(s0)); }
            m = swap_max(m);
#pragma unroll
            for (int i = 0; i < 16; ++i) { negm[i] = -m; o[i] = 0.f; }
            sc = MFMA(KF(0), qf, negm);
            for (int t = 0; t < 64; t += 2) { STEP(sc, sn, t, t + 1); STEP(sn, sc, t + 1, (t + 2 < 64 ? t + 2 : 63)); }
            l = o[8];
        }
    }
    *(u32x4*)(ws + WS_OB + ((size_t)qt * 64 + 32 + head) * 1024 + lane * 16) = pack8(o, 0, 1.0f / l);
    __syncthreads();
    { const f32x16 zero = {};
      f32x16 na = MFMA(*(const LAS bf16x8*)(lds + K2_KVA + lane * 16), asbf(ph0), zero);
      na = MFMA(*(const LAS bf16x8*)(lds + K2_KVA + 1024 + lane * 16), asbf(ph1), na);
      const float den = rbf(rbf(na[8]) + 1e-6f), rd = 1.0f / den;
#pragma unroll
      for (int i = 0; i < 8; ++i) na[i] = rbf(na[i]);
      *(u32x4*)(ws + WS_OB + ((size_t)qt * 64 + head) * 1024 + lane * 16) = pack8(na, 0, rd); }
}

__global__ __launch_bounds__(256, 1) void k3_out(const char* __restrict__ ws, unsigned short* __restrict__ out) {
    __shared__ __attribute__((aligned(16))) char red[4 * 4 * 4096];
    const int tid = threadIdx.x, lane = tid & 63, w = __builtin_amdgcn_readfirstlane(tid >> 6), r = lane & 31, h = lane >> 5;
    const int xcd = blockIdx.x & 7, bi = blockIdx.x >> 3, tb = xcd * 4 + (bi >> 3), nb = bi & 7;
    const char* ag = ws + WS_WOF + ((size_t)(w * 16) * 16 + nb * 2) * 1024 + lane * 16;
    const char* bg = ws + WS_OB + ((size_t)(tb * 2) * 64 + w * 16) * 1024 + lane * 16;
    const int tok = tb * 64 + (w & 1) * 32 + r;
    const f32x2 gg = *(const f32x2*)(ws + WS_G + (size_t)tok * 8);
    u32x4 af[16][2], bfr[16][2];
#pragma unroll
    for (int k = 0; k < 16; ++k) {
        af[k][0] = *(const u32x4*)(ag + k * 16384); bfr[k][0] = *(const u32x4*)(bg + k * 1024);
        af[k][1] = *(const u32x4*)(ag + k * 16384 + 1024); bfr[k][1] = *(const u32x4*)(bg + k * 1024 + 65536);
    }
    f32x16 acc[2][2] = {};
#pragma unroll
    for (int k = 0; k < 16; ++k)
#pragma unroll
        for (int a = 0; a < 2; ++a)
#pragma unroll
            for (int b = 0; b < 2; ++b) acc[a][b] = MFMA(asbf(af[k][a]), asbf(bfr[k][b]), acc[a][b]);
#pragma unroll
    for (int a = 0; a < 2; ++a)
#pragma unroll
        for (int b = 0; b < 2; ++b)
#pragma unroll
            for (int g = 0; g < 4; ++g) { f32x4 v = {acc[a][b][4 * g], acc[a][b][4 * g + 1], acc[a][b][4 * g + 2], acc[a][b][4 * g + 3]};
                *(f32x4*)(red + ((a * 2 + b) * 4 + w) * 4096 + g * 1024 + lane * 16) = v; }
    __syncthreads();
    unsigned short* op = out + (size_t)tok * DM + nb * 64 + (w >> 1) * 32 + 4 * h;
#pragma unroll
    for (int g = 0; g < 4; ++g) {
        const char* rp = red + w * 16384 + g * 1024 + lane * 16;
        const f32x4 a = *(const f32x4*)(rp) + *(const f32x4*)(rp + 4096), b = *(const f32x4*)(rp + 8192) + *(const f32x4*)(rp + 12288);
        float o[4];
#pragma unroll
        for (int i = 0; i < 4; ++i) o[i] = rbf(gg[0] * rbf(a[i])) + rbf(gg[1] * rbf(b[i]));
        u32x2 v = {pack2(o[0], o[1]), pack2(o[2], o[3])};
        *(u32x2*)(op + 8 * g) = v;
    }
}

extern "C" void kernel_launch(void* const* d_in, const int* in_sizes, int n_in, void* d_out, int out_size, void* d_ws, size_t ws_size, hipStream_t stream) {
    (void)in_sizes; (void)n_in; (void)out_size; (void)ws_size;
    const unsigned short* x = (const unsigned short*)d_in[0];
    const unsigned short* wA = (const unsigned short*)d_in[1];
    const unsigned short* wphi = (const unsigned short*)d_in[2];
    const unsigned short* woA = (const unsigned short*)d_in[3];
    const unsigned short* wB = (const unsigned short*)d_in[5];
    const unsigned short* woB = (const unsigned short*)d_in[6];
    const unsigned short* gw = (const unsigned short*)d_in[7];
    (void)hipFuncSetAttribute((const void*)k1_qkv, hipFuncAttributeMaxDynamicSharedMemorySize, K1_LDS);
    (void)hipFuncSetAttribute((const void*)k2_attn, hipFuncAttributeMaxDynamicSharedMemorySize, K2_LDS);
    k1_qkv<<<256, 512, K1_LDS, stream>>>(x, wA, wB, wphi, woA, woB, gw, (char*)d_ws);
    k2_attn<<<256, 512, K2_LDS, stream>>>((char*)d_ws);
    k3_out<<<256, 256, 0, stream>>>((const char*)d_ws, (unsigned short*)d_out);
}
```

```cpp
#include <hip/hip_runtime.h>
#include <hip/hip_bf16.h>
#include <stdint.h>

typedef __attribute__((ext_vector_type(2))) __bf16 bf16x2;
typedef __attribute__((ext_vector_type(8))) __bf16 bf16x8;
typedef __attribute__((ext_vector_type(16))) __bf16 bf16x16;
typedef __attribute__((ext_vector_type(2))) float f32x2;
typedef __attribute__((ext_vector_type(4))) float f32x4;
typedef __attribute__((ext_vector_type(8))) float f32x8;
typedef __attribute__((ext_vector_type(16))) float f32x16;
typedef __attribute__((ext_vector_type(2))) unsigned u32x2;
typedef __attribute__((ext_vector_type(4))) unsigned u32x4;
typedef __attribute__((ext_vector_type(4))) short s16x4;
#define LAS __attribute__((address_space(3)))
#define GAS __attribute__((address_space(1)))
#define DI __device__ __forceinline__
#define MFMA(a, b, c) __builtin_amdgcn_mfma_f32_32x32x16_bf16((a), (b), (c), 0, 0, 0)

constexpr int SEQ = 2048, DM = 512, NH = 32;
constexpr float LOG2E = 1.4426950408889634f;
constexpr size_t WS_QB = 0, WS_KB = 2u << 20, WS_VB = 4u << 20, WS_PHQ = 6u << 20, WS_KVP = 10u << 20, WS_OB = 12u << 20,
                 WS_WOF = 16u << 20, WS_G = 17u << 20;

DI unsigned pack2(float a, float b) { f32x2 v = {a, b}; return __builtin_bit_cast(unsigned, __builtin_convertvector(v, bf16x2)); }
DI float rbf(float a) { return (float)(__bf16)a; }
DI float bf2f(unsigned short b) { return __uint_as_float((unsigned)b << 16); }
DI u32x4 pack8(const f32x16& x, int o, float sc) {
    u32x4 p; p[0] = pack2(x[o] * sc, x[o + 1] * sc); p[1] = pack2(x[o + 2] * sc, x[o + 3] * sc); p[2] = pack2(x[o + 4] * sc, x[o + 5] * sc); p[3] = pack2(x[o + 6] * sc, x[o + 7] * sc); return p; }
DI u32x4 pack8(const f32x16& x, int o) {
    u32x4 p; p[0] = pack2(x[o], x[o + 1]); p[1] = pack2(x[o + 2], x[o + 3]); p[2] = pack2(x[o + 4], x[o + 5]); p[3] = pack2(x[o + 6], x[o + 7]); return p; }
DI bf16x8 asbf(u32x4 v) { return __builtin_bit_cast(bf16x8, v); }
DI float swap_max(float m) { auto r = __builtin_amdgcn_permlane32_swap(__float_as_uint(m), __float_as_uint(m), false, false); return fmaxf(__uint_as_float(r[0]), __uint_as_float(r[1])); }
DI float swap_add(float m) { auto r = __builtin_amdgcn_permlane32_swap(__float_as_uint(m), __float_as_uint(m), false, false); return __uint_as_float(r[0]) + __uint_as_float(r[1]); }
DI void glds16(const void* g, LAS char* l) { __builtin_amdgcn_global_load_lds((const GAS unsigned*)g, (LAS unsigned*)l, 16, 0, 0); }
typedef short v4i16_t __attribute__((ext_vector_type(4)));
DI s16x4 trread(const LAS char* p) { return __builtin_bit_cast(s16x4, __builtin_amdgcn_ds_read_tr16_b64_v4i16((LAS v4i16_t*)p)); }
DI bf16x8 tr2(const LAS char* p0, const LAS char* p1) { s16x4 a = trread(p0), b = trread(p1); return __builtin_bit_cast(bf16x8, __builtin_shufflevector(a, b, 0, 1, 2, 3, 4, 5, 6, 7)); }
#define STAMP_DECL
#define STAMP(i) do { } while (0)
#define STAMP_FLUSH(ws_, kid) do { } while (0)
#define WAIT_BAR(N) asm volatile("s_waitcnt vmcnt(" #N ") lgkmcnt(0)\n\ts_barrier" ::: "memory")

DI void hedgehog(const f32x16& pT, f32x16& ph) {
    float x[8];
#pragma unroll
    for (int i = 0; i < 8; ++i) x[i] = rbf(pT[i]) * LOG2E;
    float mx = fmaxf(fmaxf(fmaxf(x[0], x[1]), fmaxf(x[2], x[3])), fmaxf(fmaxf(x[4], x[5]), fmaxf(x[6], x[7])));
    float mn = fminf(fminf(fminf(x[0], x[1]), fminf(x[2], x[3])), fminf(fminf(x[4], x[5]), fminf(x[6], x[7])));
    mx = swap_max(mx); mn = -swap_max(-mn);
    float sp = 0.f, sn = 0.f;
#pragma unroll
    for (int i = 0; i < 8; ++i) { ph[i] = __builtin_amdgcn_exp2f(x[i] - mx); ph[8 + i] = __builtin_amdgcn_exp2f(mn - x[i]); sp += ph[i]; sn += ph[8 + i]; }
    sp = __builtin_amdgcn_rcpf(swap_add(sp)); sn = __builtin_amdgcn_rcpf(swap_add(sn));
#pragma unroll
    for (int i = 0; i < 8; ++i) { ph[i] *= sp; ph[8 + i] *= sn; }
}

constexpr int K1_STAGE = 40960, K1_LDS = 4 * K1_STAGE;
__global__ __launch_bounds__(512, 2) void k1_qkv(const unsigned short* __restrict__ x, const unsigned short* __restrict__ wA, const unsigned short* __restrict__ wB,
                                                const unsigned short* __restrict__ wphi, const unsigned short* __restrict__ woA, const unsigned short* __restrict__ woB,
                                                const unsigned short* __restrict__ gatew, char* __restrict__ ws) {
    extern __shared__ __attribute__((aligned(16))) char smem[];
    LAS char* lds = (LAS char*)smem;
    const int tid = threadIdx.x, lane = tid & 63, w = __builtin_amdgcn_readfirstlane(tid >> 6), r = lane & 31, h = lane >> 5;
    const int xcd = blockIdx.x & 7, bi = blockIdx.x >> 3;
    const int rb = (xcd & 1) * 8 + (bi & 7), cg = (xcd >> 1) * 4 + (bi >> 3);
    const bool isB = cg >= 8; const int hg = cg & 7;
    const unsigned short* W = isB ? wB : wA;
    const int wt = w & 3, wh = w >> 2;
    STAMP_DECL STAMP(0);
    unsigned short side[16];
    const int bidx = rb * 8 + hg;
    u32x4 gx0, gx1, gw0;
    if (isB) {
        const int fid = bidx * 8 + w, ks = fid >> 4, nt = fid & 15;
        const unsigned short* wo = (ks < 32 ? woA : woB) + (size_t)((ks & 31) * 16 + 4 * h) * DM + nt * 32 + r;
#pragma unroll
        for (int j = 0; j < 8; ++j) side[j] = wo[(size_t)((j >> 2) * 8 + (j & 3)) * DM];
        gx0 = *(const u32x4*)(x + (size_t)(bidx * 16 + 2 * w) * DM + lane * 8);
        gx1 = *(const u32x4*)(x + (size_t)(bidx * 16 + 2 * w + 1) * DM + lane * 8);
        gw0 = *(const u32x4*)(gatew + lane * 8);
    } else {
#pragma unroll
        for (int hh = 0; hh < 2; ++hh)
#pragma unroll
            for (int j = 0; j < 8; ++j) side[hh * 8 + j] = wphi[((hg * 4 + wh * 2 + hh) * 16 + (j >> 2) * 8 + 4 * h + (j & 3)) * 16 + (r & 15)];
    }
    const unsigned short* xs[2]; const unsigned short* wsrc[3];
#pragma unroll
    for (int i = 0; i < 2; ++i) { const int row = (2 * w + i) * 8 + (lane >> 3); xs[i] = x + (size_t)(rb * 128 + row) * DM + (((lane & 7) ^ ((row >> 1) & 7)) * 8); }
#pragma unroll
    for (int i = 0; i < 3; ++i) { const int p = 3 * w + i, seg = p >> 3, kr = 8 * (p & 7) + (lane >> 3);
        wsrc[i] = W + (size_t)kr * 1536 + seg * 512 + hg * 64 + (((lane & 7) ^ (((kr >> 1) & 1) << 2)) * 8); }
#define K1_STAGE_LOAD(kt) do { LAS char* sb_ = lds + ((kt) & 3) * K1_STAGE; \
        glds16(xs[0] + (kt) * 64, sb_ + (2 * w) * 1024); glds16(xs[1] + (kt) * 64, sb_ + (2 * w + 1) * 1024); \
        glds16(wsrc[0] + (size_t)(kt) * 64 * 1536, sb_ + 16384 + (3 * w) * 1024); glds16(wsrc[1] + (size_t)(kt) * 64 * 1536, sb_ + 16384 + (3 * w + 1) * 1024); \
        glds16(wsrc[2] + (size_t)(kt) * 64 * 1536, sb_ + 16384 + (3 * w + 2) * 1024); } while (0)
    K1_STAGE_LOAD(0); K1_STAGE_LOAD(1); K1_STAGE_LOAD(2); K1_STAGE_LOAD(3);
    int xoff[4];
#pragma unroll
    for (int s = 0; s < 4; ++s) xoff[s] = (32 * wt + r) * 128 + (((2 * s + h) ^ ((r >> 1) & 7)) << 4);
    const int blk = (lane >> 4) & 1, q4 = (lane & 15) >> 2, p4 = lane & 3;
    const int woff = 16384 + (8 * h + q4) * 128 + (((4 * wh + 2 * blk + (p4 >> 1)) ^ ((q4 >> 1) << 2)) << 4) + 8 * (p4 & 1);
    f32x16 aq = {}, ak = {}, av = {};
#define K1_COMPUTE(kt) do { const LAS char* sb_ = lds + ((kt) & 3) * K1_STAGE; \
        _Pragma("unroll") for (int s = 0; s < 4; ++s) { \
            const bf16x8 xf = *(const LAS bf16x8*)(sb_ + xoff[s]); \
            const LAS char* wp_ = sb_ + woff + s * 2048; \
            const bf16x8 fq = tr2(wp_, wp_ + 512), fk = tr2(wp_ + 8192, wp_ + 8192 + 512), fv = tr2(wp_ + 16384, wp_ + 16384 + 512); \
            aq = MFMA(fq, xf, aq); ak = MFMA(fk, xf, ak); av = MFMA(xf, fv, av); } } while (0)
    STAMP(1);
    WAIT_BAR(15); STAMP(2); K1_COMPUTE(0); STAMP(3);
    WAIT_BAR(10); K1_STAGE_LOAD(4); K1_COMPUTE(1);
    WAIT_BAR(10); K1_STAGE_LOAD(5); K1_COMPUTE(2);
    WAIT_BAR(10); K1_STAGE_LOAD(6); K1_COMPUTE(3);
    WAIT_BAR(10); K1_STAGE_LOAD(7); K1_COMPUTE(4);
    STAMP(4);
    WAIT_BAR(10); K1_COMPUTE(5);
    WAIT_BAR(5); K1_COMPUTE(6);
    WAIT_BAR(0); K1_COMPUTE(7);
    STAMP(5);
    const int tile = rb * 4 + wt;
    const int head0 = hg * 4 + wh * 2;
    if (isB) {
        const int sl = (r & 0x13) | ((r & 4) << 1) | ((r & 8) >> 1);
#pragma unroll
        for (int hh = 0; hh < 2; ++hh) {
            const size_t fo = ((size_t)(head0 + hh) * 64 + tile) * 1024;
            *(u32x4*)(ws + WS_QB + fo + lane * 16) = pack8(aq, 8 * hh, 0.25f * LOG2E);
            *(u32x4*)(ws + WS_KB + fo + (sl + 32 * h) * 16) = pack8(ak, 8 * hh);
        }
        { const size_t fo = ((size_t)(head0 + (r >> 4)) * 64 + tile) * 1024 + h * 256 + (r & 15) * 16;
          *(u32x4*)(ws + WS_VB + fo) = pack8(av, 0); *(u32x4*)(ws + WS_VB + fo + 512) = pack8(av, 8); }
        { u32x4 f; f[0] = side[0] | ((unsigned)side[1] << 16); f[1] = side[2] | ((unsigned)side[3] << 16); f[2] = side[4] | ((unsigned)side[5] << 16); f[3] = side[6] | ((unsigned)side[7] << 16);
          *(u32x4*)(ws + WS_WOF + (size_t)(bidx * 8 + w) * 1024 + lane * 16) = f; }
        float s0 = 0.f, s1 = 0.f;
#pragma unroll
        for (int j = 0; j < 4; ++j) {
            const float wl = __uint_as_float(gw0[j] << 16), wu = __uint_as_float(gw0[j] & 0xffff0000u);
            s0 += rbf(__uint_as_float(gx0[j] << 16) * wl) + rbf(__uint_as_float(gx0[j] & 0xffff0000u) * wu);
            s1 += rbf(__uint_as_float(gx1[j] << 16) * wl) + rbf(__uint_as_float(gx1[j] & 0xffff0000u) * wu);
        }
#pragma unroll
        for (int o = 1; o < 64; o <<= 1) { s0 += __shfl_xor(s0, o); s1 += __shfl_xor(s1, o); }
        if (lane < 2) { const float t = rbf(lane ? s1 : s0); const float g = rbf(1.f / (1.f + __expf(-t)));
            f32x2 gg = {g, rbf(1.f - g)}; *(f32x2*)(ws + WS_G + (size_t)(bidx * 16 + 2 * w + lane) * 8) = gg; }
    } else {
        LAS char* img = lds + w * 2048;
        LAS char* red = lds + 16384;
        const u32x4 vp0 = pack8(av, 0), vp1 = pack8(av, 8);
        const f32x16 zero = {};
#pragma unroll
        for (int hh = 0; hh < 2; ++hh) {
            u32x4 wf; wf[0] = side[8 * hh + 0] | ((unsigned)side[8 * hh + 1] << 16); wf[1] = side[8 * hh + 2] | ((unsigned)side[8 * hh + 3] << 16);
            wf[2] = side[8 * hh + 4] | ((unsigned)side[8 * hh + 5] << 16); wf[3] = side[8 * hh + 6] | ((unsigned)side[8 * hh + 7] << 16);
            f32x16 ph;
            f32x16 pq = MFMA(asbf(wf), asbf(pack8(aq, 8 * hh)), zero);
            f32x16 pk = MFMA(asbf(wf), asbf(pack8(ak, 8 * hh)), zero);
            hedgehog(pq, ph);
            const size_t fo = ((size_t)(head0 + hh) * 64 + tile) * 2048 + lane * 16;
            *(u32x4*)(ws + WS_PHQ + fo) = pack8(ph, 0); *(u32x4*)(ws + WS_PHQ + fo + 1024) = pack8(ph, 8);
            hedgehog(pk, ph);
#pragma unroll
            for (int g = 0; g < 4; ++g) { u32x2 v = {pack2(ph[4 * g], ph[4 * g + 1]), pack2(ph[4 * g + 2], ph[4 * g + 3])}; *(LAS u32x2*)(img + r * 64 + 8 * (2 * g + h)) = v; }
            __builtin_amdgcn_fence(__ATOMIC_RELEASE, "wavefront"); __builtin_amdgcn_wave_barrier(); __builtin_amdgcn_fence(__ATOMIC_ACQUIRE, "wavefront");
            const LAS char* tp = img + (4 * h + q4) * 64 + blk * 32 + p4 * 8;
            const bf16x8 a0 = tr2(tp, tp + 8 * 64), a1 = tr2(tp + 16 * 64, tp + 24 * 64);
            const bool mine = (r >> 4) == hh;
            u32x4 b0, b1;
#pragma unroll
            for (int j = 0; j < 4; ++j) { b0[j] = mine ? vp0[j] : 0x3F803F80u; b1[j] = mine ? vp1[j] : 0x3F803F80u; }
            f32x16 kv = MFMA(a0, asbf(b0), zero); kv = MFMA(a1, asbf(b1), kv);
            LAS char* rp = red + (((wh * 2 + hh) * 4 + wt) * 4096) + (lane ^ (hh << 4)) * 16;
#pragma unroll
            for (int g = 0; g < 4; ++g) { f32x4 v = {kv[4 * g], kv[4 * g + 1], kv[4 * g + 2], kv[4 * g + 3]}; *(LAS f32x4*)(rp + g * 1024) = v; }
            __builtin_amdgcn_fence(__ATOMIC_RELEASE, "wavefront"); __builtin_amdgcn_wave_barrier(); __builtin_amdgcn_fence(__ATOMIC_ACQUIRE, "wavefront");
        }
        __syncthreads();
#pragma unroll
        for (int i = 0; i < 2; ++i) {
            const int e = tid + i * 512, hd = e >> 8, o = (e & 255) * 16;
            const LAS char* rp = red + hd * 16384 + o;
            const f32x4 v = (*(const LAS f32x4*)(rp) + *(const LAS f32x4*)(rp + 4096)) + (*(const LAS f32x4*)(rp + 8192) + *(const LAS f32x4*)(rp + 12288));
            *(f32x4*)(ws + WS_KVP + ((size_t)(hg * 4 + hd) * 16 + rb) * 4096 + o) = v;
        }
    }
    STAMP(6); STAMP_FLUSH(ws, 0);
}

constexpr int K2_VSTR = 1120, K2_V = 65536, K2_KVA = K2_V + 64 * K2_VSTR, K2_Q = K2_KVA + 2048, K2_LDS = K2_Q + 8192;
DI f32x16 smf(bf16x8 a, const f32x16& p, int hi, f32x16 c) {
    f32x8 b = hi ? __builtin_shufflevector(p, p, 8, 9, 10, 11, 12, 13, 14, 15) : __builtin_shufflevector(p, p, 0, 1, 2, 3, 4, 5, 6, 7);
    return __builtin_amdgcn_smfmac_f32_32x32x32_bf16(a, __builtin_bit_cast(bf16x16, b), c, (int)0xDDDDDDDD, 0, 0);
}
DI float max16(const f32x16& s) {
    float a = fmaxf(fmaxf(s[0], s[1]), s[2]), b = fmaxf(fmaxf(s[3], s[4]), s[5]);
    a = fmaxf(fmaxf(a, s[6]), s[7]); b = fmaxf(fmaxf(b, s[8]), s[9]); a = fmaxf(fmaxf(a, s[10]), s[11]); b = fmaxf(fmaxf(b, s[12]), s[13]);
    a = fmaxf(fmaxf(a, s[14]), s[15]); return fmaxf(a, b);
}
__global__ __launch_bounds__(512, 2) void k2_attn(char* __restrict__ ws) {
    extern __shared__ __attribute__((aligned(16))) char smem[];
    LAS char* lds = (LAS char*)smem;
    const int tid = threadIdx.x, lane = tid & 63, w = __builtin_amdgcn_readfirstlane(tid >> 6), r = lane & 31, h = lane >> 5;
    const int xcd = blockIdx.x & 7, bi = blockIdx.x >> 3, head = xcd * 4 + (bi >> 3), qt = (bi & 7) * 8 + w;
    STAMP_DECL STAMP(0);
    const char* kg = ws + WS_KB + (size_t)head * 65536 + w * 1024 + lane * 16;
    const char* vg = ws + WS_VB + (size_t)head * 65536 + w * 1024 + lane * 16;
    glds16(ws + WS_QB + ((size_t)head * 64 + qt) * 1024 + lane * 16, lds + K2_Q + w * 1024);
#pragma unroll
    for (int c = 0; c < 8; ++c) { glds16(kg + c * 8192, lds + (c * 8 + w) * 1024); glds16(vg + c * 8192, lds + K2_V + (c * 8 + w) * K2_VSTR); }
    if (tid < 256) { const int t = tid >> 2, k = tid & 3; const unsigned v = (k & 1) ? 0u : 0x3F803F80u; u32x4 vv = {v, v, v, v};
        *(LAS u32x4*)(lds + K2_V + t * K2_VSTR + 1024 + (k & 1) * 16 + (k >> 1) * 64) = vv; }
    const bool g2 = (r >= 20 && r < 28);
    const int padoff = 1024 + (g2 ? 64 : 0) + ((r == 16 || r == 20) ? 0 : 16);
    const LAS char* kp = lds + lane * 16;
    const LAS char* vp0 = lds + K2_V + (r < 16 ? h * 256 + r * 16 : padoff);
    const LAS char* vp1 = lds + K2_V + (r < 16 ? 512 + h * 256 + r * 16 : padoff);
    STAMP(1);
    WAIT_BAR(14);
    STAMP(2);
    const bf16x8 qf = *(const LAS bf16x8*)(lds + K2_Q + w * 1024 + lane * 16);
    f32x16 negm, o = {}, sc, sn;
    { const f32x16 zero = {}; const f32x16 s0 = MFMA(*(const LAS bf16x8*)kp, qf, zero); const float m = swap_max(max16(s0));
#pragma unroll
      for (int i = 0; i < 16; ++i) negm[i] = -m; }
#define KF(t) (*(const LAS bf16x8*)(kp + (t) * 1024))
#define VF0(t) (*(const LAS bf16x8*)(vp0 + (t) * K2_VSTR))
#define VF1(t) (*(const LAS bf16x8*)(vp1 + (t) * K2_VSTR))
#define STEP(C, N, t, tn) do { const bf16x8 v0_ = VF0(t), v1_ = VF1(t); N = MFMA(KF(tn), qf, negm); \
        _Pragma("unroll") for (int i = 0; i < 8; ++i) C[i] = __builtin_amdgcn_exp2f(C[i]); \
        o = smf(v0_, C, 0, o); \
        _Pragma("unroll") for (int i = 8; i < 16; ++i) C[i] = __builtin_amdgcn_exp2f(C[i]); \
        o = smf(v1_, C, 1, o); } while (0)
#define RUN(t0, t1) do { _Pragma("unroll 2") for (int t = (t0); t < (t1); t += 2) { STEP(sc, sn, t, t + 1); STEP(sn, sc, t + 1, (t + 2 < 64 ? t + 2 : 63)); } } while (0)
    sc = MFMA(KF(0), qf, negm);
    RUN(0, 6);
    STAMP(3);
    WAIT_BAR(10);
    STAMP(4);
    RUN(6, 22);
    STAMP(5);
    WAIT_BAR(0);
    STAMP(6);
    f32x2 part[16];
#pragma unroll
    for (int i = 0; i < 16; ++i) part[i] = *(const f32x2*)(ws + WS_KVP + ((size_t)head * 16 + i) * 4096 + tid * 8);
    const u32x4 ph0 = *(const u32x4*)(ws + WS_PHQ + ((size_t)head * 64 + qt) * 2048 + lane * 16);
    const u32x4 ph1 = *(const u32x4*)(ws + WS_PHQ + ((size_t)head * 64 + qt) * 2048 + 1024 + lane * 16);
    RUN(22, 38);
    STAMP(7);
    { f32x2 a = ((part[0] + part[1]) + (part[2] + part[3])) + ((part[4] + part[5]) + (part[6] + part[7]));
      f32x2 b = ((part[8] + part[9]) + (part[10] + part[11])) + ((part[12] + part[13]) + (part[14] + part[15]));
      a += b;
      const int g = tid >> 7, ln = (tid & 127) >> 1, pr = tid & 1;
      *(LAS unsigned*)(lds + K2_KVA + (g >> 1) * 1024 + ln * 16 + ((g & 1) * 2 + pr) * 4) = pack2(a[0], a[1]); }
    STAMP(8);
    RUN(38, 64);
    STAMP(9);
    float l = o[8];
    {
        float chk = fabsf(o[0]);
#pragma unroll
        for (int i = 1; i < 8; ++i) chk = fmaxf(chk, fabsf(o[i]));
        const bool bad = !(l < 1.8446744e19f) || !(chk < 3.0e38f);
        if (__builtin_expect(__any(bad), 0)) {
            const f32x16 zero = {}; float m = -INFINITY;
            for (int t = 0; t < 64; ++t) { const f32x16 s0 = MFMA(KF(t), qf, zero); m = fmaxf(m, max16(s0)); }
            m = swap_max(m);
#pragma unroll
            for (int i = 0; i < 16; ++i) { negm[i] = -m; o[i] = 0.f; }
            sc = MFMA(KF(0), qf, negm);
            for (int t = 0; t < 64; t += 2) { STEP(sc, sn, t, t + 1); STEP(sn, sc, t + 1, (t + 2 < 64 ? t + 2 : 63)); }
            l = o[8];
        }
    }
    *(u32x4*)(ws + WS_OB + ((size_t)qt * 64 + 32 + head) * 1024 + lane * 16) = pack8(o, 0, 1.0f / l);
    __syncthreads();
    { const f32x16 zero = {};
      f32x16 na = MFMA(*(const LAS bf16x8*)(lds + K2_KVA + lane * 16), asbf(ph0), zero);
      na = MFMA(*(const LAS bf16x8*)(lds + K2_KVA + 1024 + lane * 16), asbf(ph1), na);
      const float den = rbf(rbf(na[8]) + 1e-6f), rd = 1.0f / den;
#pragma unroll
      for (int i = 0; i < 8; ++i) na[i] = rbf(na[i]);
      *(u32x4*)(ws + WS_OB + ((size_t)qt * 64 + head) * 1024 + lane * 16) = pack8(na, 0, rd); }
    STAMP(10); STAMP_FLUSH(ws, 1);
}

__global__ __launch_bounds__(256, 1) void k3_out(const char* __restrict__ ws, unsigned short* __restrict__ out) {
    __shared__ __attribute__((aligned(16))) char red[4 * 4 * 4096];
    const int tid = threadIdx.x, lane = tid & 63, w = __builtin_amdgcn_readfirstlane(tid >> 6), r = lane & 31, h = lane >> 5;
    const int xcd = blockIdx.x & 7, bi = blockIdx.x >> 3, tb = xcd * 4 + (bi >> 3), nb = bi & 7;
    STAMP_DECL STAMP(0);
    const char* ag = ws + WS_WOF + ((size_t)(w * 16) * 16 + nb * 2) * 1024 + lane * 16;
    const char* bg = ws + WS_OB + ((size_t)(tb * 2) * 64 + w * 16) * 1024 + lane * 16;
    const int tok = tb * 64 + (w & 1) * 32 + r;
    const f32x2 gg = *(const f32x2*)(ws + WS_G + (size_t)tok * 8);
    u32x4 af[16][2], bfr[16][2];
#pragma unroll
    for (int k = 0; k < 16; ++k) {
        af[k][0] = *(const u32x4*)(ag + k * 16384); bfr[k][0] = *(const u32x4*)(bg + k * 1024);
        af[k][1] = *(const u32x4*)(ag + k * 16384 + 1024); bfr[k][1] = *(const u32x4*)(bg + k * 1024 + 65536);
    }
    f32x16 acc[2][2] = {};
#pragma unroll
    for (int k = 0; k < 16; ++k)
#pragma unroll
        for (int a = 0; a < 2; ++a)
#pragma unroll
            for (int b = 0; b < 2; ++b) acc[a][b] = MFMA(asbf(af[k][a]), asbf(bfr[k][b]), acc[a][b]);
#pragma unroll
    for (int a = 0; a < 2; ++a)
#pragma unroll
        for (int b = 0; b < 2; ++b)
#pragma unroll
            for (int g = 0; g < 4; ++g) { f32x4 v = {acc[a][b][4 * g], acc[a][b][4 * g + 1], acc[a][b][4 * g + 2], acc[a][b][4 * g + 3]};
                *(f32x4*)(red + ((a * 2 + b) * 4 + w) * 4096 + g * 1024 + lane * 16) = v; }
    STAMP(1);
    __syncthreads();
    STAMP(2);
    unsigned short* op = out + (size_t)tok * DM + nb * 64 + (w >> 1) * 32 + 4 * h;
#pragma unroll
    for (int g = 0; g < 4; ++g) {
        const char* rp = red + w * 16384 + g * 1024 + lane * 16;
        const f32x4 a = *(const f32x4*)(rp) + *(const f32x4*)(rp + 4096), b = *(const f32x4*)(rp + 8192) + *(const f32x4*)(rp + 12288);
        float o[4];
#pragma unroll
        for (int i = 0; i < 4; ++i) o[i] = rbf(gg[0] * rbf(a[i])) + rbf(gg[1] * rbf(b[i]));
        u32x2 v = {pack2(o[0], o[1]), pack2(o[2], o[3])};
        *(u32x2*)(op + 8 * g) = v;
    }
    STAMP(3); STAMP_FLUSH(ws, 2);
}

extern "C" void kernel_launch(void* const* d_in, const int* in_sizes, int n_in, void* d_out, int out_size, void* d_ws, size_t ws_size, hipStream_t stream) {
    (void)in_sizes; (void)n_in; (void)out_size; (void)ws_size;
    const unsigned short* x = (const unsigned short*)d_in[0];
    const unsigned short* wA = (const unsigned short*)d_in[1];
    const unsigned short* wphi = (const unsigned short*)d_in[2];
    const unsigned short* woA = (const unsigned short*)d_in[3];
    const unsigned short* wB = (const unsigned short*)d_in[5];
    const unsigned short* woB = (const unsigned short*)d_in[6];
    const unsigned short* gw = (const unsigned short*)d_in[7];
    (void)hipFuncSetAttribute((const void*)k1_qkv, hipFuncAttributeMaxDynamicSharedMemorySize, K1_LDS);
    (void)hipFuncSetAttribute((const void*)k2_attn, hipFuncAttributeMaxDynamicSharedMemorySize, K2_LDS);
    k1_qkv<<<256, 512, K1_LDS, stream>>>(x, wA, wB, wphi, woA, woB, gw, (char*)d_ws);
    k2_attn<<<256, 512, K2_LDS, stream>>>((char*)d_ws);
    k3_out<<<256, 256, 0, stream>>>((const char*)d_ws, (unsigned short*)d_out);
}
```

```cpp
#include <hip/hip_runtime.h>
#include <hip/hip_bf16.h>
#include <stdint.h>

typedef __attribute__((ext_vector_type(2))) __bf16 bf16x2;
typedef __attribute__((ext_vector_type(8))) __bf16 bf16x8;
typedef __attribute__((ext_vector_type(16))) __bf16 bf16x16;
typedef __attribute__((ext_vector_type(2))) float f32x2;
typedef __attribute__((ext_vector_type(4))) float f32x4;
typedef __attribute__((ext_vector_type(8))) float f32x8;
typedef __attribute__((ext_vector_type(16))) float f32x16;
typedef __attribute__((ext_vector_type(2))) unsigned u32x2;
typedef __attribute__((ext_vector_type(4))) unsigned u32x4;
typedef __attribute__((ext_vector_type(4))) short s16x4;
typedef unsigned short u16;
#define LAS __attribute__((address_space(3)))
#define GAS __attribute__((address_space(1)))
#define DI __device__ __forceinline__
#define MFMA(a, b, c) __builtin_amdgcn_mfma_f32_32x32x16_bf16((a), (b), (c), 0, 0, 0)
#define SB() __builtin_amdgcn_sched_barrier(0)
#define PIN(x) asm volatile("" : "+v"(x))

constexpr int DM = 512;
constexpr float LOG2E = 1.4426950408889634f;
constexpr size_t WS_QB = 0, WS_KB = 2u << 20, WS_VB = 4u << 20, WS_PHQ = 6u << 20, WS_KVP = 10u << 20, WS_OB = 12u << 20,
                 WS_G = 16u << 20,
                 WS_E = WS_G + 65536,
                 WS_FA = WS_E + 4096,
                 WS_FB = WS_FA + 65536,
                 WS_F2 = WS_FB + 65536;
constexpr int NREP = 32, POLL_SLEEP = 4;

DI unsigned pack2(float a, float b) { f32x2 v = {a, b}; return __builtin_bit_cast(unsigned, __builtin_convertvector(v, bf16x2)); }
DI float rbf(float a) { return (float)(__bf16)a; }
DI u32x4 pack8(const f32x16& x, int o, float sc) {
    u32x4 p; p[0] = pack2(x[o] * sc, x[o + 1] * sc); p[1] = pack2(x[o + 2] * sc, x[o + 3] * sc); p[2] = pack2(x[o + 4] * sc, x[o + 5] * sc); p[3] = pack2(x[o + 6] * sc, x[o + 7] * sc); return p; }
DI u32x4 pack8(const f32x16& x, int o) {
    u32x4 p; p[0] = pack2(x[o], x[o + 1]); p[1] = pack2(x[o + 2], x[o + 3]); p[2] = pack2(x[o + 4], x[o + 5]); p[3] = pack2(x[o + 6], x[o + 7]); return p; }
DI u32x4 noff(u32x4 v) { return u32x4{min(v[0], ~1u), min(v[1], ~1u), min(v[2], ~1u), min(v[3], ~1u)}; }
DI bf16x8 asbf(u32x4 v) { return __builtin_bit_cast(bf16x8, v); }
DI float swap_max(float m) { auto r = __builtin_amdgcn_permlane32_swap(__float_as_uint(m), __float_as_uint(m), false, false); return fmaxf(__uint_as_float(r[0]), __uint_as_float(r[1])); }
#define DPP_ADD(v, ctrl) v += __int_as_float(__builtin_amdgcn_update_dpp(0, __float_as_int(v), ctrl, 0xf, 0xf, true))
DI void glds16(const void* sbase, unsigned voff, unsigned ldsb) {
    asm volatile("s_mov_b32 m0, %2\n\ts_nop 0\n\tglobal_load_lds_dwordx4 %0, %1" :: "v"(voff), "s"(sbase), "s"(ldsb) : "memory");
}
DI void glds16c(const void* sbase, unsigned voff, unsigned ldsb) {
    asm volatile("s_mov_b32 m0, %2\n\ts_nop 0\n\tglobal_load_lds_dwordx4 %0, %1 sc1" :: "v"(voff), "s"(sbase), "s"(ldsb) : "memory");
}
DI void glds4(const void* sbase, unsigned voff, unsigned ldsb) {
    asm volatile("s_mov_b32 m0, %2\n\ts_nop 0\n\tglobal_load_lds_dword %0, %1" :: "v"(voff), "s"(sbase), "s"(ldsb) : "memory");
}
typedef __amdgpu_buffer_rsrc_t rsrc_t;
DI rsrc_t ws_rsrc(const void* ws) { return __builtin_amdgcn_make_buffer_rsrc((void*)ws, 0, 0x2000000, 0x00020000); }
DI void st16(rsrc_t rs, unsigned soff, unsigned voff, u32x4 v) { __builtin_amdgcn_raw_buffer_store_b128(v, rs, voff + soff, 0, 16); }
DI u32x4 ld16(rsrc_t rs, unsigned off) { return __builtin_amdgcn_raw_buffer_load_b128(rs, off, 0, 16); }
typedef unsigned long long u64;
DI u64 flag_ld(const char* p) { return __hip_atomic_load((const GAS u64*)p, __ATOMIC_RELAXED, __HIP_MEMORY_SCOPE_AGENT); }
DI void flag_st(char* p, u64 v) { __hip_atomic_store((GAS u64*)p, v, __ATOMIC_RELAXED, __HIP_MEMORY_SCOPE_AGENT); }
DI void flag_wait(const char* p, u64 tag) {
    int spin = 0;
    while (!__all(flag_ld(p) == tag) && ++spin < (1 << 20)) __builtin_amdgcn_s_sleep(POLL_SLEEP);
    asm volatile("" ::: "memory");
}
typedef short v4i16_t __attribute__((ext_vector_type(4)));
DI s16x4 trread(const LAS char* p) { return __builtin_bit_cast(s16x4, __builtin_amdgcn_ds_read_tr16_b64_v4i16((LAS v4i16_t*)p)); }
DI bf16x8 tr2(const LAS char* p0, const LAS char* p1) { s16x4 a = trread(p0), b = trread(p1); return __builtin_bit_cast(bf16x8, __builtin_shufflevector(a, b, 0, 1, 2, 3, 4, 5, 6, 7)); }
#define WAIT_BAR(N) asm volatile("s_waitcnt vmcnt(" #N ") lgkmcnt(0)\n\ts_barrier" ::: "memory")

#define KEEP(x) asm volatile("" :: "v"(x))
#define BAR_L() asm volatile("s_waitcnt lgkmcnt(0)\n\ts_barrier" ::: "memory")
#define WAIT_V(N) asm volatile("s_waitcnt vmcnt(" #N ")" ::: "memory")

constexpr int K1_STAGE = 40960, LDS_BYTES = 163840;
constexpr int K2_VSTR = 1120, K2_REC = 2048 + 2 * K2_VSTR, K2_L = 32 * K2_REC, K2_Q = K2_L + 2048, K2_X = K2_Q + 8192;
constexpr int K2_CNT = 7 * K2_REC + 2048 + 1056;
static_assert(K2_CNT >= 32768 && K2_CNT + 16 <= 40960, "cnt");
static_assert(K2_X + 8 * 2048 <= LDS_BYTES, "lds");
DI f32x16 smf(bf16x8 a, const f32x16& p, int hi, f32x16 c) {
    f32x8 b = hi ? __builtin_shufflevector(p, p, 8, 9, 10, 11, 12, 13, 14, 15) : __builtin_shufflevector(p, p, 0, 1, 2, 3, 4, 5, 6, 7);
    return __builtin_amdgcn_smfmac_f32_32x32x32_bf16(a, __builtin_bit_cast(bf16x16, b), c, (int)0xDDDDDDDD, 0, 0);
}
DI float max16(const f32x16& s) {
    float a = fmaxf(fmaxf(s[0], s[1]), s[2]), b = fmaxf(fmaxf(s[3], s[4]), s[5]);
    a = fmaxf(fmaxf(a, s[6]), s[7]); b = fmaxf(fmaxf(b, s[8]), s[9]); a = fmaxf(fmaxf(a, s[10]), s[11]); b = fmaxf(fmaxf(b, s[12]), s[13]);
    a = fmaxf(fmaxf(a, s[14]), s[15]); return fmaxf(a, b);
}

__global__ __launch_bounds__(512, 2) void fused_fwd(const u16* __restrict__ x, const u16* __restrict__ wA, const u16* __restrict__ wB, const u16* __restrict__ wphi,
                                                   const u16* __restrict__ woA, const u16* __restrict__ woB, const u16* __restrict__ gatew,
                                                   char* __restrict__ ws, u16* __restrict__ out) {
    extern __shared__ __attribute__((aligned(16))) char smem[];
    LAS char* lds = (LAS char*)smem;
    const unsigned lds0 = (unsigned)(uintptr_t)lds;
    const int tid = threadIdx.x, lane = tid & 63, w = __builtin_amdgcn_readfirstlane(tid >> 6), r = lane & 31, h = lane >> 5;
    const int xcd = blockIdx.x & 7, bi = blockIdx.x >> 3;
    u32x4 cpf0, cpf1;
    {
        const char* pc; unsigned len;
        asm volatile("s_getpc_b64 %0\n.Lpf_base:\n\ts_mov_b32 %1, .Lcode_end-.Lpf_base-16" : "=s"(pc), "=s"(len));
        const unsigned o0 = tid * 16u, o1 = min(8192u + (bi % 3) * 8192u + tid * 16u, len & ~15u);
        asm volatile("global_load_dwordx4 %0, %2, %4\n\tglobal_load_dwordx4 %1, %3, %4" : "=&v"(cpf0), "=&v"(cpf1) : "v"(o0), "v"(o1), "s"(pc) : "memory");
    }
    const int blk = (lane >> 4) & 1, q4 = (lane & 15) >> 2, p4 = lane & 3;
    const unsigned l16 = lane * 16;
    const f32x16 zero = {};
    u64 tag;
    const unsigned my_row = ((xcd * 32 + (bi & (NREP - 1))) * 32 + r) * 8;
    unsigned my_flag;
    const int tb = xcd * 4 + (bi >> 3);
    const unsigned oo = WS_OB + tb * 131072 + (w >> 1) * 16384 + (w & 1) * 1024 + l16;
    u32x4 of[8], gx, gw; u32x2 gg;
    f32x16 oA, oB; u64 lf;
    const LAS char *kp, *vp0, *vp1;
    const unsigned ob_off = WS_OB + ((((bi & 7) * 4 + (w & 3)) * 64 + 32 + tb) * 2 + (w >> 2)) * 1024 + l16;
    u32x4 late[4]; unsigned late_off, late_str; bool kvjob;
    {
    const int rb = (xcd & 1) * 8 + (bi & 7), cg = (xcd >> 1) * 4 + (bi >> 3);
    const bool isQ = cg & 1; const int hg = cg >> 1;
    const int wt = w & 3, wh = w >> 2;
    const char* sx = (const char*)x + (size_t)rb * 128 * 1024;
    const char* sw0 = (const char*)wA + (isQ ? 0 : 1024) + hg * 128;
    const char* sw1 = (const char*)wB + (isQ ? 1024 : 0) + hg * 128;
    const char* sw2 = (const char*)(isQ ? wB : wA) + 2048 + hg * 128;
    unsigned vx[2];
#pragma unroll
    for (int i = 0; i < 2; ++i) { const int row = (2 * w + i) * 8 + (lane >> 3); vx[i] = row * 1024 + (((lane & 7) ^ ((row >> 1) & 7)) << 4); }
    const unsigned vw = (8 * w + (lane >> 3)) * 3072 + (((lane & 7) ^ (((lane >> 4) & 1) << 2)) << 4);
    const int rot = ((bi & 7) + 2 * (bi >> 3)) % 7;
#define KROT(kt) ((kt) == 0 ? 0 : 1 + ((kt) - 1 + rot >= 7 ? (kt) - 8 + rot : (kt) - 1 + rot))
#define K1_PIECE(kt, i) do { const unsigned sb_ = lds0 + ((kt) & 3) * K1_STAGE; const int kr_ = KROT(kt); \
        if ((i) == 0) glds16(sx + kr_ * 128, vx[0], sb_ + (2 * w) * 1024); if ((i) == 1) glds16(sx + kr_ * 128, vx[1], sb_ + (2 * w + 1) * 1024); \
        if ((i) == 2) glds16(sw0 + kr_ * 196608, vw, sb_ + 16384 + w * 1024); if ((i) == 3) glds16(sw1 + kr_ * 196608, vw, sb_ + 24576 + w * 1024); \
        if ((i) == 4) glds16(sw2 + kr_ * 196608, vw, sb_ + 32768 + w * 1024); } while (0)
#define K1_STAGE_LOAD(kt) do { K1_PIECE(kt, 0); K1_PIECE(kt, 1); K1_PIECE(kt, 2); K1_PIECE(kt, 3); K1_PIECE(kt, 4); } while (0)
    K1_STAGE_LOAD(0);
    K1_STAGE_LOAD(1);
    unsigned pf;
    {
        const char* p = ws + (size_t)(w + 8 * (lane & 1)) * (lane & 1 ? 0u : (2u << 20)) + (lane & 1 ? (16u << 20) : 0u);
        if (lane & 1) { if (w == 1) p = (const char*)out; if (w == 2) p = (const char*)out + (2u << 20) - 64; if (w == 3) p = (const char*)woA; if (w == 4) p = (const char*)woB; if (w == 5) p = (const char*)gatew; if (w == 6) p = (const char*)wphi; }
        asm volatile("global_load_dword %0, %1, off" : "=v"(pf) : "v"(p) : "memory");
    }
    const int grow = ((bi & 7) * 8 + (w & 3) * 2 + (w >> 2)) * 32 + tb;
    asm volatile("global_load_dwordx4 %0, %2, %3\n\tglobal_load_dwordx4 %1, %2, %4" : "=&v"(gx), "=&v"(gw) : "v"(l16), "s"((const char*)x + (size_t)grow * 1024), "s"(gatew) : "memory");
    int xoff[4];
#pragma unroll
    for (int s = 0; s < 4; ++s) xoff[s] = (32 * wt + r) * 128 + (((2 * s + h) ^ ((r >> 1) & 7)) << 4);
    const int woff = 16384 + (8 * h + q4) * 128 + (((4 * wh + 2 * blk + (p4 >> 1)) ^ ((q4 >> 1) << 2)) << 4) + 8 * (p4 & 1);
    f32x16 a0 = {}, a1 = {}, a2 = {};
    bf16x8 fa[4][4], fb[4][4];
#define K1_READ(F, kt, s) do { const LAS char* sb_ = lds + ((kt) & 3) * K1_STAGE; const LAS char* wp_ = sb_ + woff + (s) * 2048; \
        F[s][0] = *(const LAS bf16x8*)(sb_ + xoff[s]); if ((kt) < 4 || (kt) == 7) F[s][1] = tr2(wp_, wp_ + 512); F[s][2] = tr2(wp_ + 8192, wp_ + 8192 + 512); F[s][3] = tr2(wp_ + 16384, wp_ + 16384 + 512); } while (0)
#define K1_READ0(F, i, kt, s) do { const LAS char* sb_ = lds + ((kt) & 3) * K1_STAGE; const LAS char* wp_ = sb_ + woff + (s) * 2048; \
        F[s][i] = *(const LAS bf16x8*)(sb_ + xoff[s]); F[s][(i) + 1] = tr2(wp_, wp_ + 512); } while (0)
#define K1_MMA(F, kt, s) do { if ((kt) < 4) { a0 = MFMA(F[s][1], F[s][0], a0); PIN(a0); } a1 = MFMA(F[s][2], F[s][0], a1); a2 = MFMA(F[s][0], F[s][3], a2); PIN(a1); PIN(a2); } while (0)
#define K1_ITER(F, G, kt) do { _Pragma("unroll") for (int s = 0; s < 4; ++s) { K1_MMA(F, kt, s); K1_READ(G, (kt) + 1, s); SB(); \
        if ((kt) < 4) { K1_PIECE((kt) + 4, s); if (s == 3) K1_PIECE((kt) + 4, 4); SB(); } \
        if ((kt) == 1 && s == 0) { GATE0(); SB(); } if ((kt) == 1 && s == 1) { GATE1(); SB(); } if ((kt) == 1 && s == 2) { GATE2(); SB(); } } } while (0)
    float gs;
#define GATE0() do { gs = 0.f; _Pragma("unroll") for (int j = 0; j < 4; ++j) { const unsigned ux = gx[j], uw = gw[j]; gs = __builtin_amdgcn_fdot2_f32_bf16(__builtin_bit_cast(bf16x2, ux), __builtin_bit_cast(bf16x2, uw), gs, false); } \
        DPP_ADD(gs, 0xB1); DPP_ADD(gs, 0x4E); PIN(gs); } while (0)
#define GATE1() do { DPP_ADD(gs, 0x141); DPP_ADD(gs, 0x140); \
        { auto q = __builtin_amdgcn_permlane16_swap(__float_as_uint(gs), __float_as_uint(gs), false, false); gs = __uint_as_float(q[0]) + __uint_as_float(q[1]); } \
        { auto q = __builtin_amdgcn_permlane32_swap(__float_as_uint(gs), __float_as_uint(gs), false, false); gs = __uint_as_float(q[0]) + __uint_as_float(q[1]); } PIN(gs); } while (0)
#define GATE2() do { const float g = rbf(__builtin_amdgcn_rcpf(1.f + __builtin_amdgcn_exp2f(-rbf(gs) * LOG2E))); gg = u32x2{__float_as_uint(g), __float_as_uint(rbf(1.f - g))}; PIN(gg); } while (0)
    WAIT_BAR(8);
    KEEP(cpf0); KEEP(cpf1);
    K1_READ(fa, 0, 0); K1_READ(fa, 0, 1); K1_READ(fa, 0, 2); K1_READ(fa, 0, 3); SB();
    K1_STAGE_LOAD(2); K1_STAGE_LOAD(3);
    WAIT_BAR(13);
    K1_ITER(fa, fb, 0);
    WAIT_BAR(10); KEEP(pf); PIN(gx); PIN(gw); K1_ITER(fb, fa, 1);
    WAIT_BAR(10); K1_ITER(fa, fb, 2);
    WAIT_BAR(10); K1_ITER(fb, fa, 3);
    WAIT_BAR(10); glds4((const char*)wphi + hg * 2048 + w * 256, lane * 4, lds0 + 24576 + w * 256);
    tag = flag_ld(ws + WS_E);
    if (tid == 0) *(LAS u32x4*)(lds + K2_CNT) = u32x4{0u, 0u, 0u, 0u};
    st16(ws_rsrc(ws), 0, ob_off, u32x4{~0u, ~0u, ~0u, ~0u}); st16(ws_rsrc(ws), 0, ob_off - 65536, u32x4{~0u, ~0u, ~0u, ~0u});
    __builtin_amdgcn_raw_buffer_store_b64(gg, ws_rsrc(ws), lane == 0 ? (unsigned)(WS_G + grow * 8) : 0x80000000u, 0, 16); SB();
    K1_ITER(fa, fb, 4);
    WAIT_BAR(10); K1_ITER(fb, fa, 5);
    WAIT_BAR(3); PIN(tag); K1_ITER(fa, fb, 6);
    tag = tag * 0x9E3779B97F4A7C15ull + 0xD1B54A32D192ED03ull;
#pragma unroll
    for (int s = 0; s < 4; ++s) { K1_MMA(fb, 7, s); K1_READ0(fa, 0, 4, s); K1_READ0(fa, 2, 5, s); SB(); }
#pragma unroll
    for (int s = 0; s < 4; ++s) a0 = MFMA(fb[s][1], fb[s][0], a0);
    PIN(a0); SB();
    const rsrc_t rs = ws_rsrc(ws);
    const int head0 = hg * 4 + wh * 2;
    const int tile = rb * 4 + wt;
    my_flag = ((hg * 32 + r) * 32 + rb * 2 + (isQ ? 1 : 0)) * 8;
    const u32x4 vq0 = pack8(a2, 0), vq1 = pack8(a2, 8);
    {
        const int sl = (r & 0x13) | ((r & 4) << 1) | ((r & 8) >> 1);
        const unsigned so = (isQ ? WS_KB : WS_QB) + (head0 * 64 + tile) * 1024, vo = (isQ ? sl + 32 * h : lane) * 16;
        const float sc = isQ ? 1.0f : 0.25f * LOG2E;
        st16(rs, so, vo, pack8(a1, 0, sc)); st16(rs, so + 65536, vo, pack8(a1, 8, sc));
        const unsigned vo2 = isQ ? (unsigned)WS_VB + (head0 * 64 + tile) * 1024 + (r >> 4) * 65536 + h * 256 + (r & 15) * 16 : 0x80000000u;
        st16(rs, 0, vo2, vq0); st16(rs, 512, vo2, vq1);
    }
    SB();
    u32x4 wc;
    {   const int u = 2 * blk + (p4 >> 1), uq = isQ ? ((u >> 1) | ((u & 1) << 1)) : u;
        const LAS char* wfp = lds + 24576 + (wh * 2 + (p4 & 1)) * 512 + (4 * h + q4) * 32 + uq * 8;
        wc = __builtin_bit_cast(u32x4, tr2(wfp, wfp + 256)); }
#pragma unroll
    for (int s = 0; s < 4; ++s) { K1_READ0(fb, 0, 6, s); a0 = MFMA(fa[s][1], fa[s][0], a0); a0 = MFMA(fa[s][3], fa[s][2], a0); PIN(a0); SB(); }
#pragma unroll
    for (int s = 0; s < 4; ++s) a0 = MFMA(fb[s][1], fb[s][0], a0);
    PIN(a0); SB();
    WAIT_BAR(0);
    if (w == 7 && lane < NREP) flag_st(ws + WS_FA + my_flag, tag);
    f32x16 p;
    {   const bool hi = (r >> 2) & 1; u32x4 t0, t1;
#pragma unroll
        for (int j = 0; j < 4; ++j) { t0[j] = hi ? 0u : wc[j]; t1[j] = hi ? wc[j] : 0u; }
        p = MFMA(asbf(t0), asbf(pack8(a0, 0)), zero); p = MFMA(asbf(t1), asbf(pack8(a0, 8)), p); PIN(p);
        const int padoff = 1024 + ((r >= 20 && r < 28) ? 64 : 0) + ((r == 16 || r == 20) ? 0 : 16);
        kp = lds + wh * 1024 + lane * 16;
        vp0 = lds + 2048 + wh * K2_VSTR + (r < 16 ? h * 256 + r * 16 : padoff);
        vp1 = lds + 2048 + wh * K2_VSTR + (r < 16 ? 512 + h * 256 + r * 16 : padoff);
        oA = zero; oB = zero;
        PIN(kp); PIN(vp0); PIN(vp1); PIN(oA); PIN(oB); SB(); }
    if (tid >= 256) { const int t = (tid & 255) >> 2, k = tid & 3; const unsigned v = (k & 1) ? 0u : 0x3F803F80u; u32x4 vv = {v, v, v, v};
        *(LAS u32x4*)(lds + (t >> 1) * K2_REC + 2048 + (t & 1) * K2_VSTR + 1024 + (k & 1) * 16 + (k >> 1) * 64) = vv; }
    if (!isQ) { LAS char* vd = lds + (12 + 2 * w) * K2_REC + 2048; *(LAS u32x4*)(vd + lane * 16) = vq0; *(LAS u32x4*)(vd + K2_REC + lane * 16) = vq1; }
    PIN(p);
    u32x4 pp[2], pn[2];
    {
        f32x2 xx[8];
#pragma unroll
        for (int i = 0; i < 8; ++i) xx[i] = f32x2{p[2 * i], p[2 * i + 1]};
        float mx = fmaxf(xx[0][0], xx[0][1]), mn = fminf(xx[0][0], xx[0][1]);
#pragma unroll
        for (int i = 1; i < 8; ++i) { mx = fmaxf(fmaxf(mx, xx[i][0]), xx[i][1]); mn = fminf(fminf(mn, xx[i][0]), xx[i][1]); }
        f32x2 ep[8], en[8];
#pragma unroll
        for (int i = 0; i < 8; ++i) {
            const f32x2 a = (xx[i] - mx) * LOG2E, b = (mn - xx[i]) * LOG2E;
            ep[i] = f32x2{__builtin_amdgcn_exp2f(a[0]), __builtin_amdgcn_exp2f(a[1])}; en[i] = f32x2{__builtin_amdgcn_exp2f(b[0]), __builtin_amdgcn_exp2f(b[1])};
        }
        const f32x2 sp2 = ((ep[0] + ep[1]) + (ep[2] + ep[3])) + ((ep[4] + ep[5]) + (ep[6] + ep[7]));
        const f32x2 sn2 = ((en[0] + en[1]) + (en[2] + en[3])) + ((en[4] + en[5]) + (en[6] + en[7]));
        const float rp = __builtin_amdgcn_rcpf(sp2[0] + sp2[1]), rn = __builtin_amdgcn_rcpf(sn2[0] + sn2[1]);
#pragma unroll
        for (int i = 0; i < 8; ++i) { const f32x2 a = ep[i] * rp, b = en[i] * rn; pp[i >> 2][i & 3] = pack2(a[0], a[1]); pn[i >> 2][i & 3] = pack2(b[0], b[1]); }
        PIN(pp[0]); PIN(pp[1]); PIN(pn[0]); PIN(pn[1]);
    }
    late[0] = pp[0]; late[1] = pp[1]; late[2] = pn[0]; late[3] = pn[1];
    kvjob = !isQ && wh == 1;
    if (isQ) { late_off = WS_PHQ + (head0 * 64 + tile) * 2048 + h * 131072 + r * 16; late_str = 512; }
    else {
        LAS char* ip = lds + (12 + 2 * w + h) * K2_REC + r * 64; const int crot = (r >> 1) & 3;
        *(LAS u32x4*)(ip + ((0 ^ crot) << 4)) = pp[0]; *(LAS u32x4*)(ip + ((1 ^ crot) << 4)) = pp[1]; *(LAS u32x4*)(ip + ((2 ^ crot) << 4)) = pn[0]; *(LAS u32x4*)(ip + ((3 ^ crot) << 4)) = pn[1];
        asm volatile("s_waitcnt lgkmcnt(0)" ::: "memory");
        if (lane == 0) __hip_atomic_fetch_add((LAS unsigned*)(lds + K2_CNT + 8), 1u, __ATOMIC_RELAXED, __HIP_MEMORY_SCOPE_WORKGROUP);
        late_off = !wh ? 0x80000000u : (unsigned)WS_KVP + ((hg * 4 + wt) * 16 + rb) * 4096 + (lane ^ ((wt & 1) << 4)) * 16; late_str = 1024;
    }
    if (wh == 0) {
        flag_wait(ws + WS_FA + my_row, tag);
        asm volatile("" ::: "memory");
        const char* kg = ws + WS_KB + (size_t)tb * 65536 + w * 1024; const char* vg = ws + WS_VB + (size_t)tb * 65536 + w * 1024;
        const char* qg = ws + WS_QB + ((size_t)tb * 64 + (bi & 7) * 8 + 2 * w) * 1024;
        glds16c(kg, l16, lds0 + w * K2_REC); glds16c(qg, l16, lds0 + K2_Q + w * 2048); glds16c(qg + 1024, l16, lds0 + K2_Q + w * 2048 + 1024);
        glds16c(kg + 32768, l16, lds0 + w * K2_REC + 1024); glds16c(vg, l16, lds0 + w * K2_REC + 2048); glds16c(vg + 32768, l16, lds0 + w * K2_REC + 2048 + K2_VSTR);
    }
    }
    {
    const int head = xcd * 4 + (bi >> 3), qb = bi & 7;
    const int wq = w & 3, kh = w >> 2;
    const int qt = qb * 8 + wq * 2 + kh;
    const rsrc_t rs = ws_rsrc(ws);
    const int t0 = 32 * kh + wq;
    const char* kg = ws + WS_KB + (size_t)head * 65536 + t0 * 1024;
    const char* vg = ws + WS_VB + (size_t)head * 65536 + t0 * 1024;
#define K2_CHUNK(c) do { glds16c(kg + (c) * 4096, l16, lds0 + (wq + 4 * (c)) * K2_REC + kh * 1024); glds16c(vg + (c) * 4096, l16, lds0 + (wq + 4 * (c)) * K2_REC + 2048 + kh * K2_VSTR); } while (0)
    const LAS char* qp = lds + K2_Q + (wq * 2) * 1024 + lane * 16;
    LAS char* mp = lds + K2_L + wq * 512 + lane * 8;
    if (kh == 0) {
        WAIT_V(3);
        const bf16x8 kz = *(const LAS bf16x8*)(lds + wq * K2_REC + lane * 16);
        const f32x16 s0 = MFMA(kz, *(const LAS bf16x8*)(qp), zero), s1 = MFMA(kz, *(const LAS bf16x8*)(qp + 1024), zero);
        *(LAS f32x2*)mp = f32x2{-swap_max(max16(s0)), -swap_max(max16(s1))};
    }
    if (kvjob) {
        const int hh = wq & 1, trow = 4 * h + q4;
        const LAS char* tp = lds + (12 + (wq >> 1) * 8 + hh) * K2_REC + trow * 64 + (((2 * blk + (p4 >> 1)) ^ ((trow >> 1) & 3)) << 4) + (p4 & 1) * 8;
        const LAS char* vp = lds + (12 + (wq >> 1) * 8) * K2_REC + 2048 + ((r >> 4) == hh ? lane * 16 : 1024);
        while (__hip_atomic_load((LAS unsigned*)(lds + K2_CNT + 8), __ATOMIC_RELAXED, __HIP_MEMORY_SCOPE_WORKGROUP) < 8u) __builtin_amdgcn_s_sleep(1);
        asm volatile("" ::: "memory");
        bf16x8 ta[8], tb[8];
#pragma unroll
        for (int i = 0; i < 8; ++i) { ta[i] = tr2(tp + (i >> 1) * 2 * K2_REC + (i & 1) * 1024, tp + (i >> 1) * 2 * K2_REC + (i & 1) * 1024 + 512); tb[i] = *(const LAS bf16x8*)(vp + i * K2_REC); }
        SB();
        f32x16 kv = zero;
#pragma unroll
        for (int i = 0; i < 8; ++i) kv = MFMA(ta[i], tb[i], kv);
#pragma unroll
        for (int g = 0; g < 4; ++g) late[g] = __builtin_bit_cast(u32x4, f32x4{kv[4 * g], kv[4 * g + 1], kv[4 * g + 2], kv[4 * g + 3]});
    }
    f32x16 mA, mB, sA, sB;
    bf16x8 k0, k1, v00, v01, v10, v11;
    unsigned sidx = 0xDDDDDDDDu; PIN(sidx);
    WAIT_BAR(0);
    const f32x2 mm = *(const LAS f32x2*)mp;
    const bf16x8 qA = *(const LAS bf16x8*)(qp), qB = *(const LAS bf16x8*)(qp + 1024);
    k0 = *(const LAS bf16x8*)kp; v00 = *(const LAS bf16x8*)vp0; v10 = *(const LAS bf16x8*)vp1;
    SB();
    K2_CHUNK(1); K2_CHUNK(2);
    __builtin_amdgcn_s_setprio(3);
#define BUMP() do { kp += 16 * K2_REC; vp0 += 16 * K2_REC; vp1 += 16 * K2_REC; } while (0)
#define SM1 "v_smfmac_f32_32x32x32_bf16 %3, %6, v[88:95], %14\n\t"
#define STEP_TEXT(M1, G1a, G1b, G2a, G2b, G3a, G3b) \
        "v_exp_f32 v64, v64\n\t" "v_exp_f32 v65, v65\n\t"  \
        M1 \
        "ds_read_b128 %4, %15 offset:%c18\n\t" "ds_read_b128 %5, %16 offset:%c18\n\t" \
        "v_exp_f32 v66, v66\n\t" "v_exp_f32 v67, v67\n\t" "v_exp_f32 v68, v68\n\t"  \
        "v_mfma_f32_32x32x16_bf16 v[80:95], %7, %11, %13\n\t" \
        "ds_read_b128 %6, %17 offset:%c18\n\t" \
        "v_exp_f32 v69, v69\n\t" "v_exp_f32 v70, v70\n\t" "v_exp_f32 v71, v71\n\t" "v_exp_f32 v72, v72\n\t" G1a "v_exp_f32 v73, v73\n\t"  \
        "v_smfmac_f32_32x32x32_bf16 %2, %8, v[64:71], %14\n\t" G1b \
        "v_exp_f32 v74, v74\n\t" "v_exp_f32 v75, v75\n\t" "v_exp_f32 v76, v76\n\t" "v_exp_f32 v77, v77\n\t" "v_exp_f32 v78, v78\n\t" "v_exp_f32 v79, v79\n\t"  \
        "v_exp_f32 v80, v80\n\t" G2a "v_exp_f32 v81, v81\n\t"  \
        "v_smfmac_f32_32x32x32_bf16 %2, %9, v[72:79], %14\n\t" G2b \
        "v_exp_f32 v82, v82\n\t" "v_exp_f32 v83, v83\n\t" "v_exp_f32 v84, v84\n\t"  \
        "s_waitcnt lgkmcnt(0)\n\t" \
        "v_mfma_f32_32x32x16_bf16 v[64:79], %4, %10, %12\n\t" \
        "v_exp_f32 v85, v85\n\t" "v_exp_f32 v86, v86\n\t" "v_exp_f32 v87, v87\n\t" "v_exp_f32 v88, v88\n\t" G3a "v_exp_f32 v89, v89\n\t"  \
        "v_smfmac_f32_32x32x32_bf16 %3, %8, v[80:87], %14\n\t" G3b \
        "v_exp_f32 v90, v90\n\t" "v_exp_f32 v91, v91\n\t" "v_exp_f32 v92, v92\n\t" "v_exp_f32 v93, v93\n\t" "v_exp_f32 v94, v94\n\t" "v_exp_f32 v95, v95\n\t"
#define STEP_OUT(kn, v0n, v1p) "+{v[64:79]}"(sA), "+{v[80:95]}"(sB), "+v"(oA), "+v"(oB), "=&v"(kn), "=&v"(v0n), "+v"(v1p)
#define STEP_IN(t1, kc, v0c, v1c) "v"(kc), "v"(v0c), "v"(v1c), "v"(qA), "v"(qB), "{v[96:111]}"(mA), "{v[112:127]}"(mB), "v"(sidx), "v"(kp), "v"(vp0), "v"(vp1), "i"(((t1) & 15) * K2_REC)
#define STEPX(M1, t1, kc, kn, v0c, v0n, v1c, v1p) asm volatile(STEP_TEXT(M1, "", "", "", "", "", "") : STEP_OUT(kn, v0n, v1p) : STEP_IN(t1, kc, v0c, v1c))
#define STEP(t1, kc, kn, v0c, v0n, v1c, v1p) STEPX(SM1, t1, kc, kn, v0c, v0n, v1c, v1p)
#define STEPG2(SC, vo, g1, l1, g2, l2, t1, kc, kn, v0c, v0n, v1c, v1p) asm volatile(STEP_TEXT(SM1, "s_mov_b32 m0, %21\n\t", "global_load_lds_dwordx4 %19, %20" SC "\n\t", "", "", \
        "s_mov_b32 m0, %23\n\t", "global_load_lds_dwordx4 %19, %22" SC "\n\t") : STEP_OUT(kn, v0n, v1p) : STEP_IN(t1, kc, v0c, v1c), "v"(vo), "s"(g1), "s"(l1), "s"(g2), "s"(l2) : "memory")
#define STEPG3(SC, vo, g1, l1, g2, l2, g3, l3, t1, kc, kn, v0c, v0n, v1c, v1p) asm volatile(STEP_TEXT(SM1, "s_mov_b32 m0, %21\n\t", "global_load_lds_dwordx4 %19, %20" SC "\n\t", \
        "s_mov_b32 m0, %23\n\t", "global_load_lds_dwordx4 %19, %22" SC "\n\t", "s_mov_b32 m0, %25\n\t", "global_load_lds_dwordx4 %19, %24" SC "\n\t") \
        : STEP_OUT(kn, v0n, v1p) : STEP_IN(t1, kc, v0c, v1c), "v"(vo), "s"(g1), "s"(l1), "s"(g2), "s"(l2), "s"(g3), "s"(l3) : "memory")
#define RA k0, k1, v00, v01, v10, v11
#define RB k1, k0, v01, v00, v11, v10
#define CALL(M, ...) M(__VA_ARGS__)
#define KC(c) kg + (c) * 4096, lds0 + (wq + 4 * (c)) * K2_REC + kh * 1024
#define VC(c) vg + (c) * 4096, lds0 + (wq + 4 * (c)) * K2_REC + 2048 + kh * K2_VSTR
    asm volatile("v_mov_b32 v96, %5\n\tv_mov_b32 v97, %5\n\t" "v_pk_mov_b32 v[98:99], v[96:97], v[96:97] op_sel:[0,1]\n\t" "v_pk_mov_b32 v[100:101], v[96:97], v[96:97] op_sel:[0,1]\n\t" "v_pk_mov_b32 v[102:103], v[96:97], v[96:97] op_sel:[0,1]\n\t" "v_pk_mov_b32 v[104:105], v[96:97], v[96:97] op_sel:[0,1]\n\t" "v_pk_mov_b32 v[106:107], v[96:97], v[96:97] op_sel:[0,1]\n\t" "v_pk_mov_b32 v[108:109], v[96:97], v[96:97] op_sel:[0,1]\n\t" "v_pk_mov_b32 v[110:111], v[96:97], v[96:97] op_sel:[0,1]\n\t"
                 "v_mov_b32 v112, %6\n\tv_mov_b32 v113, %6\n\t"
                 "v_mfma_f32_32x32x16_bf16 v[64:79], %7, %8, v[96:111]\n\t"
                 "v_pk_mov_b32 v[114:115], v[112:113], v[112:113] op_sel:[0,1]\n\t" "v_pk_mov_b32 v[116:117], v[112:113], v[112:113] op_sel:[0,1]\n\t" "v_pk_mov_b32 v[118:119], v[112:113], v[112:113] op_sel:[0,1]\n\t" "v_pk_mov_b32 v[120:121], v[112:113], v[112:113] op_sel:[0,1]\n\t" "v_pk_mov_b32 v[122:123], v[112:113], v[112:113] op_sel:[0,1]\n\t" "v_pk_mov_b32 v[124:125], v[112:113], v[112:113] op_sel:[0,1]\n\t" "v_pk_mov_b32 v[126:127], v[112:113], v[112:113] op_sel:[0,1]\n\t"  "s_nop 4"
                 : "=&{v[64:79]}"(sA), "=&{v[80:95]}"(sB), "=&{v[96:111]}"(mA), "=&{v[112:127]}"(mB), "=&v"(v11) : "v"(mm[0]), "v"(mm[1]), "v"(k0), "v"(qA));
#define PRIO(n) __builtin_amdgcn_s_setprio(n)
#define STA(t) STEP((t) + 1, k0, k1, v00, v01, v10, v11)
#define STB(t) STEP((t) + 1, k1, k0, v01, v00, v11, v10)
#define ARRIVE(cnt, fl) do { unsigned old_ = 0; if (lane == 0) old_ = __hip_atomic_fetch_add((LAS unsigned*)(lds + (cnt)), 1u, __ATOMIC_RELAXED, __HIP_MEMORY_SCOPE_WORKGROUP); \
        if (__builtin_amdgcn_readfirstlane(old_) == 7 && lane < NREP) flag_st(fl, tag); } while (0)
    PRIO(2); STEPX("s_nop 0\n\t", 1, k0, k1, v00, v01, v10, v11); PRIO(1); STB(1); PRIO(0); STA(2);
    WAIT_BAR(0);
    u32x4 wz[2];
    const char* wzp = (const char*)woB + bi * 16384 + w * 2048 + l16;
    PRIO(3);
    CALL(STEPG3, " sc1", l16, KC(3), VC(3), KC(4), 4, RB); CALL(STEPG3, " sc1", l16, VC(4), KC(5), VC(5), 5, RA);
    CALL(STEPG2, " sc1", l16, KC(6), VC(6), 6, RB); PRIO(2); CALL(STEPG2, " sc1", l16, KC(7), VC(7), 7, RA);
    wz[0] = *(const u32x4*)(wzp); wz[1] = *(const u32x4*)(wzp + 1024);
    st16(rs, 0, late_off, late[0]); SB();
    STB(7); st16(rs, late_str, late_off, late[1]); SB();
    PRIO(1); STA(8); st16(rs, 2 * late_str, late_off, late[2]); SB();
    STB(9); st16(rs, 3 * late_str, late_off, late[3]); SB();
    PRIO(0); STA(10);
    WAIT_BAR(6);
    PRIO(3);
    if (w == 3 && lane < NREP) flag_st(ws + WS_F2 + ((qb * 32 + r) * 32 + head) * 8, tag);
    STB(11); STA(12); STB(13);
    WAIT_V(0); ARRIVE(K2_CNT, ws + WS_FB + my_flag);
    KEEP(wz[0]); KEEP(wz[1]);
    STA(14); BUMP(); STB(15); STA(16); PRIO(2); STB(17);
    lf = flag_ld(ws + WS_FB + my_row); SB();
    STA(18); STB(19);
    PIN(lf);
    if (__builtin_expect(!__all(lf == tag), 0)) flag_wait(ws + WS_FB + my_row, tag);
    asm volatile("" ::: "memory");
    const unsigned pbase = WS_KVP + (head * 16 + kh * 8) * 4096 + (tid & 255) * 16;
    u32x4 part[8], ph0, ph1;
    part[0] = ld16(rs, pbase); part[1] = ld16(rs, pbase + 4096); part[2] = ld16(rs, pbase + 2 * 4096); part[3] = ld16(rs, pbase + 3 * 4096); SB();
    STA(20); part[4] = ld16(rs, pbase + 4 * 4096); part[5] = ld16(rs, pbase + 5 * 4096); part[6] = ld16(rs, pbase + 6 * 4096); part[7] = ld16(rs, pbase + 7 * 4096); SB();
    PRIO(1); STB(21); ph0 = ld16(rs, WS_PHQ + (head * 64 + qt) * 2048 + l16); ph1 = ld16(rs, WS_PHQ + (head * 64 + qt) * 2048 + 1024 + l16);
    lf = flag_ld(ws + WS_F2 + my_row); SB();
    STA(22); PRIO(0); STB(23);
#pragma unroll
    for (int i = 0; i < 8; ++i) PIN(part[i]);
#define PF4(i) __builtin_bit_cast(f32x4, part[i])
    {
        const f32x4 hs = ((PF4(0) + PF4(1)) + (PF4(2) + PF4(3))) + ((PF4(4) + PF4(5)) + (PF4(6) + PF4(7)));
        *(LAS u32x2*)(lds + K2_Q + (w >> 1) * 1024 + lane * 16 + (w & 1) * 8) = u32x2{pack2(hs[0], hs[1]), pack2(hs[2], hs[3])}; }
    BAR_L();
    PRIO(3);
    const int nb = bi & 7;
    const unsigned vw3 = (8 * w + (lane >> 3)) * 1024 + (((lane & 7) ^ (((lane >> 4) & 1) << 2)) << 4);
#define WO_B(i) glds16((const char*)woB + nb * 128 + (i) * 65536, vw3, lds0 + 65536 + (i) * 8192 + w * 1024)
#define WA(i) (const char*)woA + nb * 128 + (i) * 65536, lds0 + (i) * 8192 + w * 1024
#define WB(i) (const char*)woB + nb * 128 + (i) * 65536, lds0 + 65536 + (i) * 8192 + w * 1024
    bf16x8 kq[4];
#pragma unroll
    for (int g = 0; g < 4; ++g) kq[g] = *(const LAS bf16x8*)(lds + K2_Q + g * 1024 + lane * 16);
    PIN(ph0); PIN(ph1); PIN(lf);
    if (__builtin_expect(!__all(lf == tag), 0)) flag_wait(ws + WS_F2 + my_row, tag);
    asm volatile("" ::: "memory");
    CALL(STEPG3, "", vw3, WA(0), WA(1), WA(2), 25, RA);
    f32x16 na;
    PIN(kq[0]); PIN(kq[1]); PIN(kq[2]); PIN(kq[3]);
    na = MFMA(kq[0], asbf(ph0), zero); na = MFMA(kq[1], asbf(ph1), na); na = MFMA(kq[2], asbf(ph0), na); na = MFMA(kq[3], asbf(ph1), na);
    PIN(na);
    CALL(STEPG3, "", vw3, WA(3), WA(4), WA(5), 26, RB);
    { PIN(na); const float rd = __builtin_amdgcn_rcpf(na[8] + 1e-6f);
      st16(rs, 0, ob_off - 65536, noff(pack8(na, 0, rd))); }
    SB();
    PRIO(2); CALL(STEPG3, "", vw3, WA(6), WA(7), WB(0), 27, RA); CALL(STEPG2, "", vw3, WB(1), WB(2), 28, RB); PRIO(1); CALL(STEPG2, "", vw3, WB(3), WB(4), 29, RA); STB(29);
    STEP(31, k0, k1, v00, v01, v10, v11); PRIO(0);
#pragma unroll
    for (int k = 0; k < 8; ++k) of[k] = ld16(rs, oo + k * 2048);
    gg = __builtin_amdgcn_raw_buffer_load_b64(rs, WS_G + (tb * 64 + (w & 1) * 32 + r) * 8, 0, 16);
    SB();
    STEP(31, k1, k0, v01, v00, v11, v10);
    asm volatile("s_nop 1" : "+{v[80:95]}"(sB));
    oB = smf(v11, sB, 1, oB);
    asm volatile("s_nop 11" : "+v"(oA), "+v"(oB));
    { const f32x16 snd = kh ? oA : oB; LAS char* xp = lds + K2_X + w * 2048 + lane * 16;
      *(LAS f32x4*)(xp) = f32x4{snd[0], snd[1], snd[2], snd[3]}; *(LAS f32x4*)(xp + 1024) = f32x4{snd[4], snd[5], snd[6], snd[7]};
      *(LAS float*)(lds + K2_L + w * 256 + lane * 4) = snd[8]; }
    WAIT_BAR(9);
    f32x16 o = kh ? oB : oA;
    { const LAS char* xp = lds + K2_X + (w ^ 4) * 2048 + lane * 16; const f32x4 a = *(const LAS f32x4*)(xp), b = *(const LAS f32x4*)(xp + 1024);
      const float lo = *(const LAS float*)(lds + K2_L + (w ^ 4) * 256 + lane * 4);
#pragma unroll
      for (int k = 0; k < 8; ++k) PIN(of[k]);
      PIN(gg);
      WO_B(5); WO_B(6); WO_B(7);
#pragma unroll
      for (int i = 0; i < 4; ++i) { o[i] += a[i]; o[4 + i] += b[i]; }
      o[8] += lo; }
    {
        float chk = 0.f;
#pragma unroll
        for (int i = 0; i < 8; ++i) chk = __builtin_fmaf(fabsf(o[i]), 0.125f, chk);
        const bool bad = !(o[8] < 1.2676506e30f) || !(o[8] > 0.25f) || !(chk < 3.0e38f);
        if (__builtin_expect(__any(bad), 0)) {
            const bf16x8 qf = kh ? qB : qA;
            const unsigned ko = WS_KB + head * 65536 + l16, vo = WS_VB + head * 65536 + h * 256 + (r & 15) * 16;
            const unsigned fill = (r == 16 || r == 20) ? 0x3F803F80u : 0u; const u32x4 fill4 = {fill, fill, fill, fill};
            float m = -INFINITY;
#pragma unroll 1
            for (int t = 0; t < 64; ++t) { const f32x16 s0 = MFMA(asbf(ld16(rs, ko + t * 1024)), qf, zero); m = fmaxf(m, max16(s0)); }
            m = swap_max(m);
#pragma unroll
            for (int i = 0; i < 16; ++i) o[i] = 0.f;
#pragma unroll 1
            for (int t = 0; t < 64; ++t) {
                f32x16 s0 = MFMA(asbf(ld16(rs, ko + t * 1024)), qf, zero);
                const u32x4 va = ld16(rs, vo + t * 1024), vb = ld16(rs, vo + t * 1024 + 512);
#pragma unroll
                for (int i = 0; i < 16; ++i) s0[i] = __builtin_amdgcn_exp2f(s0[i] - m);
                o = smf(asbf(r < 16 ? va : fill4), s0, 0, o); o = smf(asbf(r < 16 ? vb : fill4), s0, 1, o);
            }
        }
    }
    st16(rs, 0, ob_off, noff(pack8(o, 0, __builtin_amdgcn_rcpf(o[8])))); SB();
    }
    {
    const int nb = bi & 7;
    const int wb = w & 1, kq = w >> 1;
    const rsrc_t rs = ws_rsrc(ws);
    const int c0 = (2 * blk + (p4 >> 1)) ^ ((q4 >> 1) << 2);
    const LAS char* ap0 = lds + kq * 16384 + (4 * h + q4) * 128 + (c0 << 4) + 8 * (p4 & 1);
    const LAS char* ap1 = lds + kq * 16384 + (4 * h + q4) * 128 + ((c0 ^ 4) << 4) + 8 * (p4 & 1);
    u16* op = out + (size_t)(tb * 64 + wb * 32 + r) * DM + nb * 64 + kq * 16 + 8 * h;
    bf16x8 fa0[8], fa1[8];
#pragma unroll
    for (int k = 0; k < 8; ++k) { fa0[k] = tr2(ap0 + k * 2048, ap0 + k * 2048 + 1024); fa1[k] = tr2(ap1 + k * 2048, ap1 + k * 2048 + 1024); }
    SB();
#define MISSING(F) ({ unsigned mx = 0; _Pragma("unroll") for (int k = 0; k < 8; ++k) mx = max(max(mx, max(F[k][0], F[k][1])), max(F[k][2], F[k][3])); __any(mx == ~0u); })
#define VERIFY(F, base) if (__builtin_expect(MISSING(F), 0)) { int spin = 0; do { \
        if (spin) __builtin_amdgcn_s_sleep(POLL_SLEEP); \
        asm volatile("" ::: "memory"); \
        _Pragma("unroll") for (int k = 0; k < 8; ++k) if (__any(max(max(F[k][0], F[k][1]), max(F[k][2], F[k][3])) == ~0u)) F[k] = ld16(rs, oo + (base) + k * 2048); \
        } while (MISSING(F) && ++spin < (1 << 20)); }
    VERIFY(of, 0)
    u32x4 ob[8];
    f32x16 acc[2][2] = {};
#pragma unroll
    for (int k = 0; k < 8; ++k) PIN(of[k]);
#pragma unroll
    for (int k = 0; k < 8; ++k) {
        acc[0][0] = MFMA(fa0[k], asbf(of[k]), acc[0][0]); acc[0][1] = MFMA(fa1[k], asbf(of[k]), acc[0][1]); PIN(acc[0][0]); PIN(acc[0][1]);
        ob[k] = ld16(rs, oo + 65536 + k * 2048); SB();
    }
    WAIT_BAR(8);
#pragma unroll
    for (int k = 0; k < 8; ++k) { fa0[k] = tr2(ap0 + 65536 + k * 2048, ap0 + 65536 + k * 2048 + 1024); fa1[k] = tr2(ap1 + 65536 + k * 2048, ap1 + 65536 + k * 2048 + 1024); }
    SB();
    VERIFY(ob, 65536)
    const float gA = __uint_as_float(gg[0]), gB = __uint_as_float(gg[1]);
#pragma unroll
    for (int k = 0; k < 8; ++k) { acc[1][0] = MFMA(fa0[k], asbf(ob[k]), acc[1][0]); acc[1][1] = MFMA(fa1[k], asbf(ob[k]), acc[1][1]); }
#pragma unroll
    for (int a = 0; a < 2; ++a) { acc[0][a] *= gA; PIN(acc[0][a]); }
#pragma unroll
    for (int a = 0; a < 2; ++a)
#pragma unroll
        for (int g = 0; g < 4; ++g) {
            const f32x4 va = {acc[0][a][4 * g], acc[0][a][4 * g + 1], acc[0][a][4 * g + 2], acc[0][a][4 * g + 3]}, vb = {acc[1][a][4 * g], acc[1][a][4 * g + 1], acc[1][a][4 * g + 2], acc[1][a][4 * g + 3]};
            *(LAS f32x4*)(lds + ((a * 2 + (g >> 1)) * 2 + wb) * 8192 + kq * 2048 + (g & 1) * 1024 + lane * 16) = va + vb * gB;
        }
    BAR_L();
    const LAS char* xr = lds + w * 8192 + lane * 16;
#define XR(i) (*(const LAS f32x4*)(xr + (i) * 1024))
    const f32x4 m0 = (XR(0) + XR(2)) + (XR(4) + XR(6)), m1 = (XR(1) + XR(3)) + (XR(5) + XR(7));
    { auto r0 = __builtin_amdgcn_permlane32_swap(pack2(m0[0], m0[1]), pack2(m1[0], m1[1]), false, false); auto r1 = __builtin_amdgcn_permlane32_swap(pack2(m0[2], m0[3]), pack2(m1[2], m1[3]), false, false);
      *(u32x4*)op = u32x4{r0[0], r1[0], r0[1], r1[1]}; }
    if (blockIdx.x == 0 && tid == 0) flag_st(ws + WS_E, tag);
    }
}

__device__ __attribute__((noinline, used)) void code_end_marker() { asm volatile(".Lcode_end:"); }

extern "C" void kernel_launch(void* const* d_in, const int* in_sizes, int n_in, void* d_out, int out_size, void* d_ws, size_t ws_size, hipStream_t stream) {
    (void)in_sizes; (void)n_in; (void)out_size; (void)ws_size;
    (void)hipFuncSetAttribute((const void*)fused_fwd, hipFuncAttributeMaxDynamicSharedMemorySize, LDS_BYTES);
    fused_fwd<<<256, 512, LDS_BYTES, stream>>>((const u16*)d_in[0], (const u16*)d_in[1], (const u16*)d_in[5], (const u16*)d_in[2], (const u16*)d_in[3], (const u16*)d_in[6],
                                              (const u16*)d_in[7], (char*)d_ws, (u16*)d_out);
}
```
